```python
import torch

def _softmax(x, axis=-1):
    return torch.softmax(x, dim=axis)

def _gelu(x):
    return torch.nn.functional.gelu(x, approximate='tanh')

def _silu(x):
    return torch.nn.functional.silu(x)

def _sigmoid(x):
    return torch.sigmoid(x)

def _layernorm(x, axis=-1, eps=1e-05):
    m = x.mean(dim=axis, keepdim=True)
    v = x.var(dim=axis, keepdim=True, unbiased=False)
    return (x - m) / torch.sqrt(v + eps)

def _scatter_add(tgt, idx, src, axis=0):
    return tgt.clone().index_add_(axis, idx, src)

def _segment_sum(src, seg, n):
    out = torch.zeros((n,) + src.shape[1:], dtype=src.dtype)
    return out.index_add_(0, seg, src)

def _pad(x, pw):
    flat = tuple((v for pair in reversed(pw) for v in pair))
    return torch.nn.functional.pad(x, flat)
kernel_name = 'adaLN_cond__spectral_fnet__dense_activation_relu__prompt'

def setup_inputs():

    _w = lambda a: (lambda t: t.to(torch.bfloat16) if t.is_floating_point() else t)(torch.as_tensor(a))

    def _A_setup():
        B, S, D = (1, 2048, 576)
        ns = torch.arange(S)[:, None] * torch.arange(S)[None, :]
        nd = torch.arange(D)[:, None] * torch.arange(D)[None, :]
        Ws_re = torch.cos(-2.0 * torch.pi * ns / S).to(float)
        Ws_im = torch.sin(-2.0 * torch.pi * ns / S).to(float)
        Wd_re = torch.cos(-2.0 * torch.pi * nd / D).to(float)
        Wd_im = torch.sin(-2.0 * torch.pi * nd / D).to(float)
        return {'x': _w((lambda *s: torch.randn(*s, dtype=torch.bfloat16) * 0.05)(B, S, D)), 'dft_seq_re': _w(Ws_re), 'dft_seq_im': _w(Ws_im), 'dft_hid_re': _w(Wd_re), 'dft_hid_im': _w(Wd_im)}
    _A_in = _A_setup()

    def _B_setup():
        B, S, D, Dff = (1, 2048, 576, 4608)
        return {'x': _w((lambda *s: torch.randn(*s, dtype=torch.bfloat16) * 0.05)(B, S, D)), 'w1': _w((lambda *s: torch.randn(*s, dtype=torch.bfloat16) * 1.6)(D, Dff)), 'w2': _w((lambda *s: torch.randn(*s, dtype=torch.bfloat16) * 1.6)(Dff, D))}
    _B_in = _B_setup()
    return {'A_x': _A_in['x'], 'A_dft_seq_re': _A_in['dft_seq_re'], 'A_dft_seq_im': _A_in['dft_seq_im'], 'A_dft_hid_re': _A_in['dft_hid_re'], 'A_dft_hid_im': _A_in['dft_hid_im'], 'B_x': _B_in['x'], 'B_w1': _B_in['w1'], 'B_w2': _B_in['w2'], 'c': _w((lambda *s: torch.randn(*s, dtype=torch.bfloat16) * 0.1)(1, 576) * 8.0), 'w_mod': _w((lambda *s: torch.randn(*s, dtype=torch.bfloat16) * 0.1)(576, 6 * 576) * 576 ** (-0.5) * 8.0)}

def reference(A_x, A_dft_seq_re, A_dft_seq_im, A_dft_hid_re, A_dft_hid_im, B_x, B_w1, B_w2, c, w_mod):

    def _A_ref(x, dft_seq_re, dft_seq_im, dft_hid_re, dft_hid_im):
        a = torch.einsum('bsd,ed->bse', x, dft_hid_re)
        b = torch.einsum('bsd,ed->bse', x, dft_hid_im)
        y = torch.einsum('ts,bse->bte', dft_seq_re, a) - torch.einsum('ts,bse->bte', dft_seq_im, b)
        return y

    def _B_ref(x, w1, w2):
        h = torch.einsum('bsd,df->bsf', x, w1)
        zero = torch.zeros_like(h)
        h = torch.maximum(h, zero)
        return torch.einsum('bsf,fd->bsd', h, w2)
    x = A_x
    mod = (_silu(c) @ w_mod).reshape(c.shape[0], 6, 1, -1)
    sa, ca, ga, sb, cb, gb = (mod[:, 0].clone(), mod[:, 1].clone(), mod[:, 2].clone(), mod[:, 3].clone(), mod[:, 4].clone(), mod[:, 5].clone())
    h = _layernorm(x, axis=-1) * (1 + ca) + sa
    x = x + ga * _A_ref(h, A_dft_seq_re, A_dft_seq_im, A_dft_hid_re, A_dft_hid_im)
    h = _layernorm(x, axis=-1) * (1 + cb) + sb
    x = x + gb * _B_ref(h, B_w1, B_w2)
    return x
```

```cpp
#include <hip/hip_runtime.h>
#include <hip/hip_bf16.h>
#include <stdint.h>
typedef unsigned short u16;
__device__ __forceinline__ float bf2f(u16 v){ return __uint_as_float(((unsigned)v)<<16); }
__device__ __forceinline__ u16 f2bf(float f){ __hip_bfloat16 b = __float2bfloat16(f); return *(u16*)&b; }
__device__ __forceinline__ float rbf(float f){ return bf2f(f2bf(f)); }
constexpr int S=2048, D=576, F=4608;

__global__ void k_mod(const u16* c, const u16* wmod, float* mod){
  int j = blockIdx.x*256+threadIdx.x; if(j>=6*D) return;
  float acc=0;
  for(int k=0;k<D;k++){ float cv=bf2f(c[k]); float s=rbf(cv/(1.f+__expf(-cv))); acc+=s*bf2f(wmod[k*6*D+j]); }
  mod[j]=rbf(acc);
}
__global__ void k_ln(const u16* x, u16* xn){
  int r=blockIdx.x; int t=threadIdx.x;
  float v[9]; float s=0;
  for(int i=0;i<9;i++){ v[i]=bf2f(x[r*D+t+64*i]); s+=v[i]; }
  for(int o=32;o;o>>=1) s+=__shfl_xor(s,o);
  float m=s/D; float q=0;
  for(int i=0;i<9;i++){ float d=v[i]-m; q+=d*d; }
  for(int o=32;o;o>>=1) q+=__shfl_xor(q,o);
  float rs=rsqrtf(q/D+1e-5f);
  for(int i=0;i<9;i++) xn[r*D+t+64*i]=f2bf((v[i]-m)*rs);
}
__global__ void k_gemm(const u16* A,int lda,const u16* B,int sbk,int sbn,float* C,int M,int N,int K){
  __shared__ float As[16][17], Bs[16][17];
  int tx=threadIdx.x&15, ty=threadIdx.x>>4;
  int m=blockIdx.y*16+ty, n=blockIdx.x*16+tx; float acc=0;
  for(int k0=0;k0<K;k0+=16){
    As[ty][tx]=bf2f(A[(long)m*lda+k0+tx]);
    Bs[ty][tx]=bf2f(B[(long)(k0+ty)*sbk+(long)n*sbn]);
    __syncthreads();
    for(int k=0;k<16;k++) acc+=As[ty][k]*Bs[k][tx];
    __syncthreads();
  }
  C[(long)m*N+n]=acc;
}
__global__ void k_rowsum(const u16* W, float* r){
  int row=blockIdx.x; float s=0; for(int i=threadIdx.x;i<S;i+=64) s+=bf2f(W[(long)row*S+i]);
  for(int o=32;o;o>>=1) s+=__shfl_xor(s,o);
  if(threadIdx.x==0) r[row]=s;
}
__global__ void k_pq(const float* P,const float* Q,const float* mod,u16* PQ){
  int i=blockIdx.x*256+threadIdx.x; if(i>=S*D) return; int t=i/D,d=i%D;
  float sc=rbf(1.f+mod[D+d]);
  PQ[t*2*D+d]=f2bf(P[i]*sc); PQ[t*2*D+D+d]=f2bf(-Q[i]*sc);
}
__global__ void k_ab(const u16* Wr,const u16* Wi,const float* mod,float* al,float* be){
  int e=blockIdx.x*64+threadIdx.x; if(e>=D) return; float a=0,b=0;
  for(int d=0;d<D;d++){ a+=bf2f(Wr[e*D+d])*mod[d]; b+=bf2f(Wi[e*D+d])*mod[d]; }
  al[e]=a; be[e]=b;
}
__global__ void k_x2(const u16* x,const float* y1,const float* y2,const float* rre,const float* rim,const float* al,const float* be,const float* mod,float* x2,u16* x2s,float* mean,float* rstd){
  int r=blockIdx.x; int t=threadIdx.x; float v[9]; float s=0;
  for(int i=0;i<9;i++){ int d=t+64*i; float y=y1[r*D+d]+y2[r*D+d]+rre[r]*al[d]-rim[r]*be[d]; v[i]=bf2f(x[r*D+d])+mod[2*D+d]*y; s+=v[i]; }
  for(int o=32;o;o>>=1) s+=__shfl_xor(s,o);
  float m=s/D; float q=0;
  for(int i=0;i<9;i++){ float d=v[i]-m; q+=d*d; }
  for(int o=32;o;o>>=1) q+=__shfl_xor(q,o);
  float rs=rsqrtf(q/D+1e-5f);
  for(int i=0;i<9;i++){ int d=t+64*i; x2[r*D+d]=v[i]; x2s[r*D+d]=f2bf(v[i]*rbf(1.f+mod[4*D+d])); }
  if(t==0){ mean[r]=m; rstd[r]=rs; }
}
__global__ void k_uv(const u16* w1,const float* mod,float* u,float* v){
  int f=blockIdx.x*64+threadIdx.x; float a=0,b=0;
  for(int d=0;d<D;d++){ float w=bf2f(w1[d*F+f]); a+=w*rbf(1.f+mod[4*D+d]); b+=w*mod[3*D+d]; }
  u[f]=a; v[f]=b;
}
__global__ void k_h1(const float* G,const float* mean,const float* rstd,const float* u,const float* v,u16* h1){
  long i=(long)blockIdx.x*256+threadIdx.x; int t=i/F,f=i%F;
  float z=rstd[t]*(G[i]-mean[t]*u[f])+v[f];
  h1[i]=f2bf(fmaxf(z,0.f));
}
__global__ void k_out(const float* x2,const float* Z,const float* mod,u16* out){
  int i=blockIdx.x*256+threadIdx.x; int d=i%D;
  out[i]=f2bf(x2[i]+mod[5*D+d]*Z[i]);
}
extern "C" void kernel_launch(void* const* d_in, const int* in_sizes, int n_in, void* d_out, int out_size, void* d_ws, size_t ws_size, hipStream_t stream){
  const u16* x=(const u16*)d_in[0]; const u16* Wsr=(const u16*)d_in[1]; const u16* Wsi=(const u16*)d_in[2];
  const u16* Wdr=(const u16*)d_in[3]; const u16* Wdi=(const u16*)d_in[4];
  const u16* w1=(const u16*)d_in[6]; const u16* w2=(const u16*)d_in[7]; const u16* c=(const u16*)d_in[8]; const u16* wmod=(const u16*)d_in[9];
  char* ws=(char*)d_ws; size_t o=0; auto al=[&](size_t b){ void* p=ws+o; o+=(b+255)&~(size_t)255; return p; };
  float* mod=(float*)al(6*D*4); u16* xn=(u16*)al(S*D*2); float* P=(float*)al(S*D*4); float* Q=(float*)al(S*D*4);
  float* rre=(float*)al(S*4); float* rim=(float*)al(S*4); u16* PQ=(u16*)al(S*D*4); float* alpha=(float*)al(D*4); float* beta=(float*)al(D*4);
  float* y1=(float*)al(S*D*4); float* y2=(float*)al(S*D*4); float* x2=(float*)al(S*D*4); u16* x2s=(u16*)al(S*D*2);
  float* mean=(float*)al(S*4); float* rstd=(float*)al(S*4); float* u=(float*)al(F*4); float* v=(float*)al(F*4);
  float* G=(float*)al((size_t)S*F*4); u16* h1=(u16*)al((size_t)S*F*2); float* Z=(float*)al(S*D*4);
  k_mod<<<(6*D+255)/256,256,0,stream>>>(c,wmod,mod);
  k_ln<<<S,64,0,stream>>>(x,xn);
  k_gemm<<<dim3(D/16,S/16),256,0,stream>>>(Wsr,S,xn,D,1,P,S,D,S);
  k_gemm<<<dim3(D/16,S/16),256,0,stream>>>(Wsi,S,xn,D,1,Q,S,D,S);
  k_rowsum<<<S,64,0,stream>>>(Wsr,rre); k_rowsum<<<S,64,0,stream>>>(Wsi,rim);
  k_pq<<<(S*D+255)/256,256,0,stream>>>(P,Q,mod,PQ);
  k_ab<<<D/64,64,0,stream>>>(Wdr,Wdi,mod,alpha,beta);
  k_gemm<<<dim3(D/16,S/16),256,0,stream>>>(PQ,2*D,Wdr,1,D,y1,S,D,D);
  k_gemm<<<dim3(D/16,S/16),256,0,stream>>>(PQ+D,2*D,Wdi,1,D,y2,S,D,D);
  k_x2<<<S,64,0,stream>>>(x,y1,y2,rre,rim,alpha,beta,mod,x2,x2s,mean,rstd);
  k_uv<<<F/64,64,0,stream>>>(w1,mod,u,v);
  k_gemm<<<dim3(F/16,S/16),256,0,stream>>>(x2s,D,w1,F,1,G,S,F,D);
  k_h1<<<(S*F)/256,256,0,stream>>>(G,mean,rstd,u,v,h1);
  k_gemm<<<dim3(D/16,S/16),256,0,stream>>>(h1,F,w2,D,1,Z,S,D,F);
  k_out<<<S*D/256,256,0,stream>>>(x2,Z,mod,(u16*)d_out);
}
```

```cpp
#include <hip/hip_runtime.h>
#include <stdint.h>

typedef unsigned short u16;
typedef __bf16 bf16x8 __attribute__((ext_vector_type(8)));
typedef __bf16 bf16x2 __attribute__((ext_vector_type(2)));
typedef float f32x4 __attribute__((ext_vector_type(4)));
typedef float f32x2 __attribute__((ext_vector_type(2)));
typedef unsigned u32x4 __attribute__((ext_vector_type(4)));
typedef unsigned u32x2 __attribute__((ext_vector_type(2)));
typedef short s16x4 __attribute__((ext_vector_type(4)));
#define LAS __attribute__((address_space(3)))
#define DEVI __device__ __forceinline__

constexpr int S = 2048, D = 576, F = 4608;
constexpr size_t WS_MODP = 0;
constexpr size_t WS_UVP  = 64 << 10;
constexpr size_t WS_STAT = 192 << 10;
constexpr size_t WS_XNT  = 1 << 20;
constexpr size_t WS_PQ   = 4 << 20;
constexpr size_t WS_X2P  = 14 << 20;
constexpr size_t WS_X2   = 17 << 20;
constexpr size_t WS_H1   = 20 << 20;
constexpr size_t WS_PART = 40 << 20;

#define WAIT_V(n) asm volatile("s_waitcnt vmcnt(%0)" ::"n"(n) : "memory")
#define WAIT_L0() do { __builtin_amdgcn_s_waitcnt(0xC07F); asm volatile("" ::: "memory"); } while (0)
#define PIN(x) asm volatile("" : "+v"(x))
#define BAR() do { asm volatile("" ::: "memory"); __builtin_amdgcn_s_barrier(); asm volatile("" ::: "memory"); } while (0)
#define SCHED() __builtin_amdgcn_sched_barrier(0)
#define SGB(m, n) __builtin_amdgcn_sched_group_barrier(m, n, 0)

#define STAMP(k, i)
DEVI float bf_lo(unsigned v) { return __uint_as_float(v << 16); }
DEVI float bf_hi(unsigned v) { return __uint_as_float(v & 0xffff0000u); }
DEVI unsigned pk2(float a, float b) { bf16x2 p = {(__bf16)a, (__bf16)b}; return __builtin_bit_cast(unsigned, p); }
DEVI float rbf(float a) { return bf_lo(pk2(a, a) & 0xffffu); }
template <int CTRL> DEVI float dpp_add(float v) {
  int t = __builtin_amdgcn_update_dpp(0, __float_as_int(v), CTRL, 0xf, 0xf, true);
  return v + __int_as_float(t);
}
DEVI float x16_add(float v) {
  u32x2 r = __builtin_amdgcn_permlane16_swap(__float_as_uint(v), __float_as_uint(v), false, false);
  return __uint_as_float(r[0]) + __uint_as_float(r[1]);
}
DEVI float x32_add(float v) {
  u32x2 r = __builtin_amdgcn_permlane32_swap(__float_as_uint(v), __float_as_uint(v), false, false);
  return __uint_as_float(r[0]) + __uint_as_float(r[1]);
}
DEVI float row16_sum(float v) { v = dpp_add<0xB1>(v); v = dpp_add<0x4E>(v); v = dpp_add<0x141>(v); v = dpp_add<0x140>(v); return v; }
DEVI void glds16(const char* g, char* l) {
 __builtin_amdgcn_global_load_lds((const unsigned*)g, (LAS unsigned*)l, 16, 0, 0); }
DEVI bf16x8 lds_frag(const char* p) { return *(const LAS bf16x8*)p; }
DEVI bf16x8 lds_frag_tr(const char* p, int off2) {
  s16x4 a = __builtin_amdgcn_ds_read_tr16_b64_v4i16((LAS s16x4*)p);
  s16x4 b = __builtin_amdgcn_ds_read_tr16_b64_v4i16((LAS s16x4*)(p + off2));
  return __builtin_bit_cast(bf16x8, __builtin_shufflevector(a, b, 0, 1, 2, 3, 4, 5, 6, 7));
}
DEVI f32x4 mfma(bf16x8 a, bf16x8 b, f32x4 c) {
 return __builtin_amdgcn_mfma_f32_16x16x32_bf16(a, b, c, 0, 0, 0); }
DEVI float mod_at(const float* modp, int i) { return rbf((modp[i] + modp[3456 + i]) + (modp[2 * 3456 + i] + modp[3 * 3456 + i])); }

__global__ __launch_bounds__(256) void k0_ln_mod(const u16* __restrict__ x, const u16* __restrict__ cvec,
                                                 const u16* __restrict__ wmod, char* __restrict__ ws) {
  __shared__ __attribute__((aligned(16))) char lds[8 * 1216 + 4 * 64 * 4];
  const int tid = threadIdx.x, lane = tid & 63, w = __builtin_amdgcn_readfirstlane(tid >> 6), b = blockIdx.x;
  STAMP(0, 0);
  const int tok = 8 * b + 2 * w + (lane >> 5), i = lane & 31;
  const unsigned* x32 = (const unsigned*)x + tok * 288 + i;
  unsigned xr[9];
#pragma unroll
  for (int n = 0; n < 9; n++) xr[n] = x32[32 * n];
  const bool domod = b < 216;
  const int cg = b % 54, kq = b / 54;
  u32x4 wv[5]; u16 cv[5];
  if (domod) {
    const int r0 = tid >> 3, ch = tid & 7;
#pragma unroll
    for (int it = 0; it < 5; it++) {
      int r = r0 + 32 * it; if (r > 143) r = 143;
      r += kq * 144;
      wv[it] = *(const u32x4*)(wmod + (size_t)r * 3456 + cg * 64 + ch * 8);
      cv[it] = cvec[r];
    }
  }
  float v[18], s = 0.f;
#pragma unroll
  for (int n = 0; n < 9; n++) { v[2 * n] = bf_lo(xr[n]); v[2 * n + 1] = bf_hi(xr[n]); s += v[2 * n] + v[2 * n + 1]; }
  STAMP(0, 1);
  s = x16_add(row16_sum(s));
  const float mean = s * (1.f / D);
  float q = 0.f;
#pragma unroll
  for (int n = 0; n < 18; n++) { v[n] -= mean; q += v[n] * v[n]; }
  q = x16_add(row16_sum(q));
  const float rs = rsqrtf(q * (1.f / D) + 1e-5f);
  {
    char* row = lds + (2 * w + (lane >> 5)) * 1216 + i * 4;
#pragma unroll
    for (int n = 0; n < 9; n++) *(LAS unsigned*)(row + 128 * n) = pk2(v[2 * n] * rs, v[2 * n + 1] * rs);
  }
  float* red = (float*)(lds + 8 * 1216);
  if (domod) {
    float a[8] = {0, 0, 0, 0, 0, 0, 0, 0};
#pragma unroll
    for (int it = 0; it < 5; it++) {
      float c = bf_lo(cv[it]);
      float sl = rbf(c / (1.f + __expf(-c)));
      if (it == 4 && (tid >> 3) >= 16) sl = 0.f;
#pragma unroll
      for (int e = 0; e < 4; e++) { a[2 * e] += sl * bf_lo(wv[it][e]); a[2 * e + 1] += sl * bf_hi(wv[it][e]); }
    }
#pragma unroll
    for (int e = 0; e < 8; e++) a[e] = x32_add(x16_add(dpp_add<0x128>(a[e])));
    if (lane < 8) {
      *(LAS f32x4*)(red + w * 64 + lane * 8) = f32x4{a[0], a[1], a[2], a[3]};
      *(LAS f32x4*)(red + w * 64 + lane * 8 + 4) = f32x4{a[4], a[5], a[6], a[7]};
    }
  }
  __syncthreads();
  STAMP(0, 2);
  if (domod && tid < 64) {
    float* modp = (float*)(ws + WS_MODP);
    modp[kq * 3456 + cg * 64 + tid] = (red[tid] + red[64 + tid]) + (red[128 + tid] + red[192 + tid]);
  }
  const int grp = lane >> 4, c = lane & 15;
  for (int it = w; it < 9; it += 4) {
    const char* p = lds + (c >> 2) * 1216 + (64 * it + 16 * grp + 4 * (c & 3)) * 2;
    bf16x8 f = lds_frag_tr(p, 4 * 1216);
    const int J = 4 * it + grp, nb = J / 9, j = J - nb * 9;
    char* dst = ws + WS_XNT + ((size_t)((nb * 64 + (b >> 2)) * 9 + j) << 10) + (((b & 3) * 16 + c) << 4);
    *(bf16x8*)dst = f;
  }
  STAMP(0, 3);
}

constexpr int NL = 2;
constexpr int NT = 256 + 64 * NL;
#define K1_MAP 0
constexpr int K1_SLOT = 34816, K1_NS = 4, K1_NST = 16;
__global__ __launch_bounds__(NT) void k1_seq(const u16* __restrict__ wsre, const u16* __restrict__ wsim, char* __restrict__ ws) {
  __shared__ __attribute__((aligned(16))) char lds[K1_NS * K1_SLOT + 1024 + 144 * 8 + 512];
  char* const ones = lds + K1_NS * K1_SLOT;
  float* const sca = (float*)(ones + 1024);
  float* const ssa = sca + 144;
  float* const srs = ssa + 144;
  const int tid = threadIdx.x, lane = tid & 63, w = __builtin_amdgcn_readfirstlane(tid >> 6);
  const int xcd = blockIdx.x & 7, idx = blockIdx.x >> 3;
  int ks, nb, mb;
  STAMP(1, 0);
  if (K1_MAP == 0) { ks = idx & 1; nb = (idx >> 1) & 3; const int ms = idx >> 3; mb = (ms >> 1) * 16 + 2 * xcd + (ms & 1); }
  else { ks = xcd & 1; nb = idx & 3; const int ms = idx >> 2; mb = (ms >> 2) * 16 + 4 * (xcd >> 1) + (ms & 3); }
  if (w >= 4) {
    const int li = w - 4;
    const char* wsb = (const char*)(mb >= 16 ? wsim : wsre) + (size_t)(mb & 15) * 128 * 4096 + ks * 2048;
    const char* xb = ws + WS_XNT + (size_t)(nb * 64 + ks * 32) * 9216;
    const unsigned vw = (lane >> 3) * 4096 + (((lane & 7) ^ (lane >> 3)) << 4), vl = lane << 4;
    auto issue = [&](int s) {
      char* slot = lds + (s % K1_NS) * K1_SLOT;
      if (li == 0) {
#pragma unroll
        for (int p = 0; p < 16; p++) glds16(wsb + s * 128 + p * 32768 + vw, slot + p * 1024);
        glds16(xb + s * 18432 + vl, slot + 16384);
      } else {
#pragma unroll
        for (int p = 1; p < 18; p++) glds16(xb + s * 18432 + p * 1024 + vl, slot + 16384 + p * 1024);
      }
    };
#pragma unroll
    for (int s = 0; s < K1_NS; s++) issue(s);
    WAIT_V(17 * (K1_NS - 1));
    BAR();
    for (int s = 0; s < K1_NST - K1_NS; s++) { WAIT_V(17 * (K1_NS - 2)); BAR(); issue(s + K1_NS); }
    WAIT_V(34); BAR(); WAIT_V(17); BAR(); WAIT_V(0); BAR();
    BAR();
    return;
  }
  const int mh = w >> 1, nh = w & 1, g = lane >> 4, c = lane & 15;
  {
    const float* modp = (const float*)(ws + WS_MODP);
    if (tid < 144) { ssa[tid] = mod_at(modp, nb * 144 + tid); sca[tid] = rbf(1.f + mod_at(modp, 576 + nb * 144 + tid)); }
    *(LAS unsigned*)(ones + tid * 4) = 0x3f803f80u;
  }
  f32x4 acc[4][5];
#pragma unroll
  for (int m = 0; m < 4; m++)
#pragma unroll
    for (int j = 0; j < 5; j++) { acc[m][j] = f32x4{0, 0, 0, 0}; PIN(acc[m][j]); }
  const int vb = (64 * mh + c) * 128 + ((g ^ (c & 7)) << 4);
  const int va = 16384 + nh * 5120 + (lane << 4);
  bf16x8 B0[4], A0[5], B1[4], A1[5];
  auto rd = [&](bf16x8* B, bf16x8* A, int s, int hk) {
    const char* slot = lds + (s % K1_NS) * K1_SLOT;
    const char* pb = slot + (vb ^ (hk << 6));
    const char* pa = slot + va + hk * 9216;
    const char* p4 = nh ? ones + (lane << 4) : pa + 4096;
    B[0] = lds_frag(pb); A[0] = lds_frag(pa);
#pragma unroll
    for (int m = 1; m < 4; m++) B[m] = lds_frag(pb + m * 2048);
#pragma unroll
    for (int j = 1; j < 4; j++) A[j] = lds_frag(pa + j * 1024);
    A[4] = lds_frag(p4);
  };
  auto mm = [&](bf16x8* B, bf16x8* A) {
#pragma unroll
    for (int j = 0; j < 5; j++)
#pragma unroll
      for (int m = 0; m < 4; m++) acc[m][j] = mfma(A[j], B[m], acc[m][j]);
  };
  auto pat = [&]() {
#pragma unroll
    for (int i = 0; i < 9; i++) { SGB(0x008, 1); SGB(0x100, 1); }
    SGB(0x008, 11);
  };
  WAIT_L0();
  BAR();
  STAMP(1, 1);
  rd(B0, A0, 0, 0);
  for (int s = 0; s < K1_NST; s++) {
    SCHED();
    rd(B1, A1, s, 1); mm(B0, A0); pat();
    SCHED();
    if (s + 1 < K1_NST) {
      WAIT_L0(); BAR();
      SCHED();
      rd(B0, A0, s + 1, 0); mm(B1, A1); pat();
    } else {
      mm(B1, A1);
    }
  }
  SCHED();
  STAMP(1, 2);
  if (nh == 1 && g == 0) {
#pragma unroll
    for (int m = 0; m < 4; m++) srs[64 * mh + 16 * m + c] = acc[m][4][0];
  }
  WAIT_L0(); BAR();
  float rsum[4];
#pragma unroll
  for (int m = 0; m < 4; m++) rsum[m] = srs[64 * mh + 16 * m + c];
  const float sgn = mb >= 16 ? -1.f : 1.f;
  const int tb = (mb & 15) * 4 + 2 * mh;
  char* const pq = ws + WS_PQ;
#pragma unroll
  for (int j = 0; j < 5; j++) {
    if (j == 4 && nh) break;
    const int jj = 5 * nh + j;
    f32x4 ca = *(const LAS f32x4*)(sca + 16 * jj + 4 * g), sa = *(const LAS f32x4*)(ssa + 16 * jj + 4 * g);
    ca *= sgn; sa *= sgn;
    const int Jp = (mb >> 4) * 36 + 9 * nb + jj;
#pragma unroll
    for (int mp = 0; mp < 2; mp++) {
      f32x4 p0 = acc[2 * mp][j] * ca + rsum[2 * mp] * sa, p1 = acc[2 * mp + 1][j] * ca + rsum[2 * mp + 1] * sa;
      u32x2 lo = __builtin_amdgcn_permlane16_swap(pk2(p0[0], p0[1]), pk2(p1[0], p1[1]), false, false);
      u32x2 hi = __builtin_amdgcn_permlane16_swap(pk2(p0[2], p0[3]), pk2(p1[2], p1[3]), false, false);
      u32x4 o = {lo[0], hi[0], lo[1], hi[1]};
      const unsigned off = (unsigned)(((((tb + mp) * 36 + (Jp >> 1)) * 2 + ks) * 2 + (g & 1)) << 10) + ((((Jp & 1) * 2 + (g >> 1)) * 16 + c) << 4);
      *(u32x4*)(pq + off) = o;
    }
  }
  STAMP(1, 3);
}

constexpr int K2_SLOT = 26624, K2_NS = 5, K2_NST = 18;
__global__ __launch_bounds__(NT) void k2_hid(const u16* __restrict__ x, const u16* __restrict__ wdre, const u16* __restrict__ wdim,
                                             const u16* __restrict__ w1, char* __restrict__ ws) {
  __shared__ __attribute__((aligned(16))) char lds[K2_NS * K2_SLOT + (144 * 2 + 192 * 2 + 512) * 4];
  float* const sga = (float*)(lds + K2_NS * K2_SLOT);
  float* const scb = sga + 144;
  float* const kcb = scb + 144;
  float* const ksb = kcb + 192;
  float* const uvr = ksb + 192;
  const int tid = threadIdx.x, lane = tid & 63, w = __builtin_amdgcn_readfirstlane(tid >> 6);
  const int xcd = blockIdx.x & 7, idx = blockIdx.x >> 3;
  const int nb = idx & 3, tb = xcd * 8 + (idx >> 2);
  STAMP(2, 0);
  if (w >= 4) {
    const int li = w - 4;
    const unsigned vw = (lane >> 3) * 1152 + (((lane & 7) ^ (lane >> 3)) << 4), vl = lane << 4;
    const char* pb = ws + WS_PQ + (size_t)tb * 36 * 4096;
    auto issue = [&](int s) {
      char* slot = lds + (s % K2_NS) * K2_SLOT;
      const char* wb = (const char*)(s >= 9 ? wdim : wdre) + nb * 144 * 1152 + (s >= 9 ? s - 9 : s) * 128;
      if (li == 0) {
#pragma unroll
        for (int p = 0; p < 13; p++) glds16(wb + p * 9216 + vw, slot + p * 1024);
      } else {
#pragma unroll
        for (int p = 0; p < 8; p++) glds16(pb + s * 8192 + p * 1024 + vl, slot + 18432 + p * 1024);
#pragma unroll
        for (int p = 13; p < 18; p++) glds16(wb + p * 9216 + vw, slot + p * 1024);
      }
    };
#pragma unroll
    for (int s = 0; s < K2_NS; s++) issue(s);
    BAR();
    WAIT_V(13 * (K2_NS - 1));
    BAR();
    for (int s = 0; s < K2_NST - K2_NS; s++) { WAIT_V(13 * (K2_NS - 2)); BAR(); issue(s + K2_NS); }
    WAIT_V(39); BAR(); WAIT_V(26); BAR(); WAIT_V(13); BAR(); WAIT_V(0); BAR();
    BAR(); BAR();
    return;
  }
  const int mh = w >> 1, kh = w & 1, g = lane >> 4, c = lane & 15;
  const int b = blockIdx.x;
  const bool douv = b < 216;
  const int cg = b % 72, th = b / 72;
  u32x4 wv[6];
  if (douv) {
#pragma unroll
    for (int it = 0; it < 6; it++) wv[it] = *(const u32x4*)(w1 + (size_t)(th * 192 + (tid >> 3) + 32 * it) * F + cg * 64 + (tid & 7) * 8);
  }
  u32x2 xr[5];
  const int tok = tb * 32 + mh * 16 + c;
#pragma unroll
  for (int jj = 0; jj < 5; jj++) {
    int j = kh * 5 + jj; if (j > 8) j = 8;
    xr[jj] = *(const u32x2*)(x + (size_t)tok * D + nb * 144 + 16 * j + 4 * g);
  }
  {
    const float* modp = (const float*)(ws + WS_MODP);
    if (tid < 144) { sga[tid] = mod_at(modp, 2 * 576 + nb * 144 + tid); scb[tid] = rbf(1.f + mod_at(modp, 4 * 576 + nb * 144 + tid)); }
    if (douv && tid < 192) { ksb[tid] = mod_at(modp, 3 * 576 + th * 192 + tid); kcb[tid] = rbf(1.f + mod_at(modp, 4 * 576 + th * 192 + tid)); }
  }
  WAIT_L0(); BAR();
  if (douv) {
    float u[8] = {0, 0, 0, 0, 0, 0, 0, 0}, v[8] = {0, 0, 0, 0, 0, 0, 0, 0};
#pragma unroll
    for (int it = 0; it < 6; it++) {
      const float a = kcb[(tid >> 3) + 32 * it], bb = ksb[(tid >> 3) + 32 * it];
#pragma unroll
      for (int e = 0; e < 4; e++) {
        const float lo = bf_lo(wv[it][e]), hi = bf_hi(wv[it][e]);
        u[2 * e] += a * lo; u[2 * e + 1] += a * hi; v[2 * e] += bb * lo; v[2 * e + 1] += bb * hi;
      }
    }
#pragma unroll
    for (int e = 0; e < 8; e++) { u[e] = x32_add(x16_add(dpp_add<0x128>(u[e]))); v[e] = x32_add(x16_add(dpp_add<0x128>(v[e]))); }
    if (lane < 8) {
      float* r = uvr + w * 128 + lane * 8;
      *(LAS f32x4*)(r) = f32x4{u[0], u[1], u[2], u[3]}; *(LAS f32x4*)(r + 4) = f32x4{u[4], u[5], u[6], u[7]};
      *(LAS f32x4*)(r + 64) = f32x4{v[0], v[1], v[2], v[3]}; *(LAS f32x4*)(r + 68) = f32x4{v[4], v[5], v[6], v[7]};
    }
  }
  f32x4 acc[9];
#pragma unroll
  for (int j = 0; j < 9; j++) { acc[j] = f32x4{0, 0, 0, 0}; PIN(acc[j]); }
  const int va = c * 128 + (((4 * kh + g) ^ (c & 7)) << 4);
  const int vb = 18432 + kh * 4096 + mh * 1024 + (lane << 4);
  bf16x8 Bx[2], By[2], A0[5], A1[4];
  auto rd0 = [&](bf16x8* B, int s) {
    const char* slot = lds + (s % K2_NS) * K2_SLOT;
    B[0] = lds_frag(slot + vb); A0[0] = lds_frag(slot + va); B[1] = lds_frag(slot + vb + 2048);
#pragma unroll
    for (int j = 1; j < 5; j++) A0[j] = lds_frag(slot + va + j * 2048);
  };
  auto rd1 = [&](int s) {
    const char* slot = lds + (s % K2_NS) * K2_SLOT;
#pragma unroll
    for (int j = 0; j < 4; j++) A1[j] = lds_frag(slot + va + (5 + j) * 2048);
  };
  auto mm0 = [&](bf16x8* B) {
#pragma unroll
    for (int j = 0; j < 5; j++) { acc[j] = mfma(A0[j], B[0], acc[j]); }
#pragma unroll
    for (int j = 0; j < 5; j++) { acc[j] = mfma(A0[j], B[1], acc[j]); }
  };
  auto mm1 = [&](bf16x8* B) {
#pragma unroll
    for (int j = 0; j < 4; j++) { acc[5 + j] = mfma(A1[j], B[0], acc[5 + j]); }
#pragma unroll
    for (int j = 0; j < 4; j++) { acc[5 + j] = mfma(A1[j], B[1], acc[5 + j]); }
  };
  auto step = [&](bf16x8* Bc, bf16x8* Bn, int s, bool last) {
    SCHED();
    rd1(s); mm0(Bc);
#pragma unroll
    for (int i = 0; i < 4; i++) { SGB(0x008, 1); SGB(0x100, 1); }
    SGB(0x008, 6);
    SCHED();
    if (!last) {
      WAIT_L0(); BAR();
      SCHED();
      rd0(Bn, s + 1); mm1(Bc);
#pragma unroll
      for (int i = 0; i < 7; i++) { SGB(0x008, 1); SGB(0x100, 1); }
      SGB(0x008, 1);
    } else mm1(Bc);
  };
  WAIT_L0(); BAR();
  STAMP(2, 1);
  rd0(Bx, 0);
  for (int s = 0; s < K2_NST - 2; s += 2) { step(Bx, By, s, false); step(By, Bx, s + 1, false); }
  step(Bx, By, K2_NST - 2, false); step(By, Bx, K2_NST - 1, true);
  SCHED();
  STAMP(2, 2);
  WAIT_L0(); BAR();
  {
    char* xb = lds + mh * 9216 + (lane << 4);
    if (kh == 0) {
#pragma unroll
      for (int j = 5; j < 9; j++) *(LAS f32x4*)(xb + j * 1024) = acc[j];
    } else {
#pragma unroll
      for (int j = 0; j < 5; j++) *(LAS f32x4*)(xb + j * 1024) = acc[j];
    }
    WAIT_L0(); BAR();
    if (kh == 0) {
#pragma unroll
      for (int j = 0; j < 5; j++) acc[j] += *(const LAS f32x4*)(xb + j * 1024);
    } else {
#pragma unroll
      for (int j = 0; j < 4; j++) acc[j] = acc[5 + j] + *(const LAS f32x4*)(xb + (5 + j) * 1024);
    }
  }
  if (douv && tid < 128) {
    float* uvp = (float*)(ws + WS_UVP);
    uvp[(th * 2 + (tid >> 6)) * F + cg * 64 + (tid & 63)] = (uvr[tid] + uvr[128 + tid]) + (uvr[256 + tid] + uvr[384 + tid]);
  }
  const int nj = 5 - kh;
  float sum = 0.f;
#pragma unroll
  for (int jj = 0; jj < 5; jj++) {
    if (jj < nj) {
      const int j = kh * 5 + jj;
      f32x4 ga = *(const LAS f32x4*)(sga + 16 * j + 4 * g), cb = *(const LAS f32x4*)(scb + 16 * j + 4 * g);
      f32x4 xv = {bf_lo(xr[jj][0]), bf_hi(xr[jj][0]), bf_lo(xr[jj][1]), bf_hi(xr[jj][1])};
      f32x4 x2 = xv + ga * acc[jj];
      acc[jj] = x2;
      sum += (x2[0] + x2[1]) + (x2[2] + x2[3]);
      u32x2 o = {pk2(x2[0], x2[1]), pk2(x2[2], x2[3])};
      *(u32x2*)(ws + WS_X2 + (((((tb * 4 + nb) * 9 + j) * 2 + mh) * 64 + lane) << 3)) = o;
      f32x4 xs = x2 * cb;
      u32x2 o2 = {pk2(xs[0], xs[1]), pk2(xs[2], xs[3])};
      const int J = 9 * nb + j;
      *(u32x2*)(ws + WS_X2P + ((((tb >> 2) * 18 + (J >> 1)) * 8 + (tb & 3) * 2 + mh) << 10) + (lane << 4) + (J & 1) * 8) = o2;
    }
  }
  sum = x32_add(x16_add(sum));
  const float lm = sum * (kh ? 1.f / 64 : 1.f / 80);
  float m2 = 0.f;
#pragma unroll
  for (int jj = 0; jj < 5; jj++) {
    if (jj < nj) {
      f32x4 d = acc[jj] - lm;
      m2 += (d[0] * d[0] + d[1] * d[1]) + (d[2] * d[2] + d[3] * d[3]);
    }
  }
  m2 = x32_add(x16_add(m2));
  if (g == 0) *(f32x2*)(ws + WS_STAT + (((nb * 2 + kh) * S + tok) << 3)) = f32x2{sum, m2};
  STAMP(2, 3);
}

constexpr int K3_SLOT = 26624, K3_NS = 5, K3_NST = 18;
__global__ __launch_bounds__(NT) void k3_up(const u16* __restrict__ w1, char* __restrict__ ws) {
  __shared__ __attribute__((aligned(16))) char lds[K3_NS * K3_SLOT + (288 * 2 + 128 * 2) * 4];
  float* const su = (float*)(lds + K3_NS * K3_SLOT);
  float* const sv = su + 288;
  float* const smean = sv + 288;
  float* const srstd = smean + 128;
  const int tid = threadIdx.x, lane = tid & 63, w = __builtin_amdgcn_readfirstlane(tid >> 6);
  const int xcd = blockIdx.x & 7, idx = blockIdx.x >> 3;
  const int mb = 8 * (xcd & 1) + (idx >> 2), fb = 4 * (xcd >> 1) + (idx & 3);
  STAMP(3, 0);
  if (w >= 4) {
    const int li = w - 4;
    const unsigned vl = lane << 4;
    const char* xb = ws + WS_X2P + (size_t)mb * 18 * 8192;
    const char* wb = (const char*)w1 + fb * 576;
    unsigned vo[13];
#pragma unroll
    for (int p = 0; p < 13; p++) {
      const int q = (li ? 13 + (p < 5 ? p : 0) : p) * 64 + lane, row = q / 36, col = q - row * 36;
      vo[p] = row * 9216 + ((col ^ (((row >> 2) & 1) << 1)) << 4);
    }
    auto issue = [&](int s) {
      char* slot = lds + (s % K3_NS) * K3_SLOT;
      const char* wbs = wb + (size_t)s * 32 * 9216;
      if (li == 0) {
#pragma unroll
        for (int p = 0; p < 13; p++) glds16(wbs + vo[p], slot + 8192 + p * 1024);
      } else {
#pragma unroll
        for (int p = 0; p < 8; p++) glds16(xb + s * 8192 + p * 1024 + vl, slot + p * 1024);
#pragma unroll
        for (int p = 0; p < 5; p++) glds16(wbs + vo[p], slot + 8192 + (13 + p) * 1024);
      }
    };
#pragma unroll
    for (int s = 0; s < K3_NS; s++) issue(s);
    WAIT_V(13 * (K3_NS - 1));
    BAR();
    for (int s = 0; s < K3_NST - K3_NS; s++) { WAIT_V(13 * (K3_NS - 2)); BAR(); issue(s + K3_NS); }
    WAIT_V(39); BAR(); WAIT_V(26); BAR(); WAIT_V(13); BAR(); WAIT_V(0); BAR();
    return;
  }
  const int mh = w >> 1, nh = w & 1, g = lane >> 4, c = lane & 15;
  {
    const float* uvp = (const float*)(ws + WS_UVP) + fb * 288;
    su[tid] = (uvp[tid] + uvp[2 * F + tid]) + uvp[4 * F + tid];
    sv[tid] = (uvp[F + tid] + uvp[3 * F + tid]) + uvp[5 * F + tid];
    if (tid < 32) {
      const int t = 256 + tid;
      su[t] = (uvp[t] + uvp[2 * F + t]) + uvp[4 * F + t];
      sv[t] = (uvp[F + t] + uvp[3 * F + t]) + uvp[5 * F + t];
    }
    if (tid >= 128) {
      const f32x2* st = (const f32x2*)(ws + WS_STAT) + mb * 128 + (tid - 128);
      f32x2 p[8]; float sum = 0.f;
#pragma unroll
      for (int i = 0; i < 8; i++) { p[i] = st[i * S]; sum += p[i][0]; }
      const float mean = sum * (1.f / D);
      float m2 = 0.f;
#pragma unroll
      for (int i = 0; i < 8; i++) {
        const float n = (i & 1) ? 64.f : 80.f, d = p[i][0] * ((i & 1) ? 1.f / 64 : 1.f / 80) - mean;
        m2 += p[i][1] + n * d * d;
      }
      smean[tid - 128] = mean; srstd[tid - 128] = rsqrtf(m2 * (1.f / D) + 1e-5f);
    }
  }
  f32x4 acc[4][9];
#pragma unroll
  for (int m = 0; m < 4; m++)
#pragma unroll
    for (int j = 0; j < 9; j++) { acc[m][j] = f32x4{0, 0, 0, 0}; PIN(acc[m][j]); }
  const int vb = mh * 4096 + (lane << 4);
  const int vae = 8192 + (4 * g + (c >> 2)) * 576 + (c & 3) * 8;
  const int sw = g & 1;
  const int vaE = vae + nh * 288 + (nh ? -32 * sw : 32 * sw);
  const int vaO = vae + nh * 288 + (nh ? 32 * sw : -32 * sw);
  bf16x8 Bx[4], By[4], A0[5], A1[4];
  auto rdA = [&](const char* slot, int j) { return lds_frag_tr(slot + ((j & 1) ? vaO : vaE) + 32 * j, 16 * 576); };
  auto rd0 = [&](bf16x8* B, int s) {
    const char* slot = lds + (s % K3_NS) * K3_SLOT;
    B[0] = lds_frag(slot + vb); A0[0] = rdA(slot, 0);
#pragma unroll
    for (int m = 1; m < 4; m++) B[m] = lds_frag(slot + vb + m * 1024);
#pragma unroll
    for (int j = 1; j < 5; j++) A0[j] = rdA(slot, j);
  };
  auto rd1 = [&](int s) {
    const char* slot = lds + (s % K3_NS) * K3_SLOT;
#pragma unroll
    for (int j = 0; j < 4; j++) A1[j] = rdA(slot, 5 + j);
  };
  auto step = [&](bf16x8* Bc, bf16x8* Bn, int s, bool last) {
    SCHED();
    rd1(s);
#pragma unroll
    for (int j = 0; j < 5; j++)
#pragma unroll
      for (int m = 0; m < 4; m++) acc[m][j] = mfma(A0[j], Bc[m], acc[m][j]);
#pragma unroll
    for (int i = 0; i < 8; i++) { SGB(0x008, 1); SGB(0x100, 1); }
    SGB(0x008, 12);
    SCHED();
    if (!last) { WAIT_L0(); BAR(); SCHED(); rd0(Bn, s + 1); }
#pragma unroll
    for (int j = 0; j < 4; j++)
#pragma unroll
      for (int m = 0; m < 4; m++) acc[m][5 + j] = mfma(A1[j], Bc[m], acc[m][5 + j]);
    if (!last) {
#pragma unroll
      for (int i = 0; i < 14; i++) { SGB(0x008, 1); SGB(0x100, 1); }
      SGB(0x008, 2);
    }
  };
  WAIT_L0(); BAR();
  STAMP(3, 1);
  rd0(Bx, 0);
  for (int s = 0; s < K3_NST - 2; s += 2) { step(Bx, By, s, false); step(By, Bx, s + 1, false); }
  step(Bx, By, K3_NST - 2, false); step(By, Bx, K3_NST - 1, true);
  SCHED();
  STAMP(3, 2);
  float mean[4], rstd[4];
#pragma unroll
  for (int m = 0; m < 4; m++) { mean[m] = smean[64 * mh + 16 * m + c]; rstd[m] = srstd[64 * mh + 16 * m + c]; }
  char* const hb = ws + WS_H1 + ((size_t)((mb * 144 + fb * 9) * 8 + 4 * mh) << 10) + (lane << 4);
  u32x2 pk[4][9];
#pragma unroll
  for (int j = 0; j < 9; j++) {
    f32x4 u = *(const LAS f32x4*)(su + 144 * nh + 16 * j + 4 * g), v = *(const LAS f32x4*)(sv + 144 * nh + 16 * j + 4 * g);
#pragma unroll
    for (int m = 0; m < 4; m++) {
      f32x4 z = rstd[m] * (acc[m][j] - mean[m] * u) + v;
      pk[m][j] = u32x2{pk2(fmaxf(z[0], 0.f), fmaxf(z[1], 0.f)), pk2(fmaxf(z[2], 0.f), fmaxf(z[3], 0.f))};
    }
  }
  if (nh == 0) {
#pragma unroll
    for (int m = 0; m < 4; m++) {
#pragma unroll
      for (int p = 0; p < 4; p++) *(u32x4*)(hb + p * 8192 + m * 1024) = u32x4{pk[m][2 * p][0], pk[m][2 * p][1], pk[m][2 * p + 1][0], pk[m][2 * p + 1][1]};
      *(u32x2*)(hb + 4 * 8192 + m * 1024) = pk[m][8];
    }
  } else {
#pragma unroll
    for (int m = 0; m < 4; m++) {
      *(u32x2*)(hb + 4 * 8192 + m * 1024 + 8) = pk[m][0];
#pragma unroll
      for (int p = 0; p < 4; p++) *(u32x4*)(hb + (5 + p) * 8192 + m * 1024) = u32x4{pk[m][2 * p + 1][0], pk[m][2 * p + 1][1], pk[m][2 * p + 2][0], pk[m][2 * p + 2][1]};
    }
  }
  STAMP(3, 3);
}

constexpr int K4_SLOT = 34816, K4_NS = 4, K4_NST = 18;
__global__ __launch_bounds__(NT) void k4_down(const u16* __restrict__ w2, char* __restrict__ ws) {
  __shared__ __attribute__((aligned(16))) char lds[K4_NS * K4_SLOT];
  const int tid = threadIdx.x, lane = tid & 63, w = __builtin_amdgcn_readfirstlane(tid >> 6);
  const int xcd = blockIdx.x & 7, idx = blockIdx.x >> 3;
  const int mb = 8 * (xcd & 1) + (idx >> 2), nb = idx & 3, ks = xcd >> 1;
  STAMP(4, 0);
  if (w >= 4) {
    const int li = w - 4;
    const unsigned vl = lane << 4;
    const char* hb = ws + WS_H1 + (size_t)(mb * 144 + ks * 36) * 8192;
    const char* wb = (const char*)w2 + (size_t)ks * 1152 * 1152 + nb * 288;
    unsigned vo[17];
#pragma unroll
    for (int p = 0; p < 17; p++) {
      const int q = (li ? p + 1 : 0) * 64 + lane, row = q / 18, col = q - row * 18;
      vo[p] = row * 1152 + (col << 4);
    }
    auto issue = [&](int s) {
      char* slot = lds + (s % K4_NS) * K4_SLOT;
      const char* wbs = wb + s * 64 * 1152;
      if (li == 0) {
#pragma unroll
        for (int p = 0; p < 16; p++) glds16(hb + s * 16384 + p * 1024 + vl, slot + p * 1024);
        glds16(wbs + vo[0], slot + 16384);
      } else {
#pragma unroll
        for (int p = 0; p < 17; p++) glds16(wbs + vo[p], slot + 16384 + (p + 1) * 1024);
      }
    };
#pragma unroll
    for (int s = 0; s < K4_NS; s++) issue(s);
    WAIT_V(17 * (K4_NS - 1));
    BAR();
    for (int s = 0; s < K4_NST - K4_NS; s++) { WAIT_V(17 * (K4_NS - 2)); BAR(); issue(s + K4_NS); }
    WAIT_V(34); BAR(); WAIT_V(17); BAR(); WAIT_V(0); BAR();
    return;
  }
  const int g = lane >> 4, c = lane & 15;
  f32x4 acc[2][9];
#pragma unroll
  for (int m = 0; m < 2; m++)
#pragma unroll
    for (int j = 0; j < 9; j++) { acc[m][j] = f32x4{0, 0, 0, 0}; PIN(acc[m][j]); }
  const int vb = w * 2048 + (lane << 4);
  const int va = 16384 + (4 * g + (c >> 2)) * 288 + (c & 3) * 8;
  bf16x8 B0[2], A0[9], B1[2], A1[9];
  auto rd = [&](bf16x8* B, bf16x8* A, int s, int hk) {
    const char* slot = lds + (s % K4_NS) * K4_SLOT;
    const char* pb = slot + vb + hk * 8192;
    const char* pa = slot + va + hk * 32 * 288;
    B[0] = lds_frag(pb); A[0] = lds_frag_tr(pa, 16 * 288); B[1] = lds_frag(pb + 1024);
#pragma unroll
    for (int j = 1; j < 9; j++) A[j] = lds_frag_tr(pa + 32 * j, 16 * 288);
  };
  auto mm = [&](bf16x8* B, bf16x8* A) {
#pragma unroll
    for (int j = 0; j < 9; j++)
#pragma unroll
      for (int m = 0; m < 2; m++) acc[m][j] = mfma(A[j], B[m], acc[m][j]);
  };
  auto pat = [&]() {
    SGB(0x008, 1); SGB(0x100, 2); SGB(0x008, 1); SGB(0x100, 2);
#pragma unroll
    for (int i = 0; i < 16; i++) { SGB(0x008, 1); SGB(0x100, 1); }
  };
  BAR();
  STAMP(4, 1);
  rd(B0, A0, 0, 0);
  for (int s = 0; s < K4_NST; s++) {
    SCHED();
    rd(B1, A1, s, 1); mm(B0, A0); pat();
    SCHED();
    if (s + 1 < K4_NST) {
      WAIT_L0(); BAR();
      SCHED();
      rd(B0, A0, s + 1, 0); mm(B1, A1); pat();
    } else {
      mm(B1, A1);
    }
  }
  SCHED();
  STAMP(4, 2);
  char* const pb = ws + WS_PART + ((size_t)((((mb * 4 + w) * 4 + nb) * 9) * 4 + ks) << 10) + (lane << 4);
#pragma unroll
  for (int j = 0; j < 9; j++)
    *(u32x4*)(pb + j * 4096) = u32x4{pk2(acc[0][j][0], acc[0][j][1]), pk2(acc[0][j][2], acc[0][j][3]), pk2(acc[1][j][0], acc[1][j][1]), pk2(acc[1][j][2], acc[1][j][3])};
  STAMP(4, 3);
}

__global__ __launch_bounds__(576) void k5_out(char* __restrict__ ws, u16* __restrict__ out) {
  const int lane = threadIdx.x & 63, j = threadIdx.x >> 6, g = lane >> 4, c = lane & 15;
  const int xcd = blockIdx.x & 7, idx = blockIdx.x >> 3;
  const int nb = idx & 3, tb = xcd * 8 + (idx >> 2);
  STAMP(5, 0);
  const int u = (tb * 4 + nb) * 9 + j;
  const char* pp = ws + WS_PART + ((size_t)u << 12) + (lane << 4);
  u32x4 p[4];
#pragma unroll
  for (int i = 0; i < 4; i++) p[i] = *(const u32x4*)(pp + i * 1024);
  u32x2 xa = *(const u32x2*)(ws + WS_X2 + ((size_t)u << 10) + (lane << 3)), xb = *(const u32x2*)(ws + WS_X2 + ((size_t)u << 10) + 512 + (lane << 3));
  const int n = nb * 144 + 16 * j + 4 * g;
  const float* mp = (const float*)(ws + WS_MODP) + 5 * 576 + n;
  f32x4 gb = (*(const f32x4*)mp + *(const f32x4*)(mp + 3456)) + (*(const f32x4*)(mp + 2 * 3456) + *(const f32x4*)(mp + 3 * 3456));
#pragma unroll
  for (int e = 0; e < 4; e++) gb[e] = rbf(gb[e]);
  STAMP(5, 1);
  float z[8];
#pragma unroll
  for (int e = 0; e < 4; e++) {
    z[2 * e] = (bf_lo(p[0][e]) + bf_lo(p[1][e])) + (bf_lo(p[2][e]) + bf_lo(p[3][e]));
    z[2 * e + 1] = (bf_hi(p[0][e]) + bf_hi(p[1][e])) + (bf_hi(p[2][e]) + bf_hi(p[3][e]));
  }
  u32x2 oa = {pk2(bf_lo(xa[0]) + gb[0] * z[0], bf_hi(xa[0]) + gb[1] * z[1]), pk2(bf_lo(xa[1]) + gb[2] * z[2], bf_hi(xa[1]) + gb[3] * z[3])};
  u32x2 ob = {pk2(bf_lo(xb[0]) + gb[0] * z[4], bf_hi(xb[0]) + gb[1] * z[5]), pk2(bf_lo(xb[1]) + gb[2] * z[6], bf_hi(xb[1]) + gb[3] * z[7])};
  STAMP(5, 2);
  *(u32x2*)(out + (size_t)(tb * 32 + c) * D + n) = oa;
  *(u32x2*)(out + (size_t)(tb * 32 + 16 + c) * D + n) = ob;
  STAMP(5, 3);
}

extern "C" void kernel_launch(void* const* d_in, const int* in_sizes, int n_in, void* d_out, int out_size, void* d_ws,
                              size_t ws_size, hipStream_t stream) {
  const u16* x = (const u16*)d_in[0];
  char* ws = (char*)d_ws;
  k0_ln_mod<<<256, 256, 0, stream>>>(x, (const u16*)d_in[8], (const u16*)d_in[9], ws);
  k1_seq<<<256, NT, 0, stream>>>((const u16*)d_in[1], (const u16*)d_in[2], ws);
  k2_hid<<<256, NT, 0, stream>>>(x, (const u16*)d_in[3], (const u16*)d_in[4], (const u16*)d_in[6], ws);
  k3_up<<<256, NT, 0, stream>>>((const u16*)d_in[6], ws);
  k4_down<<<256, NT, 0, stream>>>((const u16*)d_in[7], ws);
  k5_out<<<256, 576, 0, stream>>>(ws, (u16*)d_out);
}
```

```cpp
#include <hip/hip_runtime.h>
#include <stdint.h>

typedef unsigned short u16;
typedef __bf16 bf16x8 __attribute__((ext_vector_type(8)));
typedef __bf16 bf16x2 __attribute__((ext_vector_type(2)));
typedef float f32x4 __attribute__((ext_vector_type(4)));
typedef float f32x2 __attribute__((ext_vector_type(2)));
typedef unsigned u32x4 __attribute__((ext_vector_type(4)));
typedef unsigned u32x2 __attribute__((ext_vector_type(2)));
typedef short s16x4 __attribute__((ext_vector_type(4)));
#define LAS __attribute__((address_space(3)))
#define DEVI __device__ __forceinline__

constexpr int S = 2048, D = 576, F = 4608;
constexpr size_t WS_MODP = 0;
constexpr size_t WS_UVP  = 512 << 10;
constexpr size_t WS_STAT = 192 << 10;
constexpr size_t WS_XNT  = 1 << 20;
constexpr size_t WS_PQ   = 4 << 20;
constexpr size_t WS_RSTD = 384 << 10;
constexpr size_t WS_UV   = 400 << 10;
constexpr size_t WS_X2P  = 14 << 20;
constexpr size_t WS_X2   = 17 << 20;
constexpr size_t WS_PART = 20 << 20;

#define WAIT_V(n) asm volatile("s_waitcnt vmcnt(%0)" ::"n"(n) : "memory")
#define WAIT_L0() do { __builtin_amdgcn_s_waitcnt(0xC07F); asm volatile("" ::: "memory"); } while (0)
#define PIN(x) asm volatile("" : "+v"(x))
#define BAR() do { asm volatile("" ::: "memory"); __builtin_amdgcn_s_barrier(); asm volatile("" ::: "memory"); } while (0)
#define SCHED() __builtin_amdgcn_sched_barrier(0)
#define SGB(m, n) __builtin_amdgcn_sched_group_barrier(m, n, 0)

#define STAMP(k, i)
#define LT(i)
#define LTDECL
#define LTOUT(k)
DEVI float bf_lo(unsigned v) { return __uint_as_float(v << 16); }
DEVI float bf_hi(unsigned v) { return __uint_as_float(v & 0xffff0000u); }
DEVI unsigned pk2(float a, float b) { bf16x2 p = {(__bf16)a, (__bf16)b}; return __builtin_bit_cast(unsigned, p); }
DEVI float rbf(float a) { return bf_lo(pk2(a, a) & 0xffffu); }
template <int CTRL> DEVI float dpp_add(float v) {
  int t = __builtin_amdgcn_update_dpp(0, __float_as_int(v), CTRL, 0xf, 0xf, true);
  return v + __int_as_float(t);
}
DEVI float x16_add(float v) {
  u32x2 r = __builtin_amdgcn_permlane16_swap(__float_as_uint(v), __float_as_uint(v), false, false);
  return __uint_as_float(r[0]) + __uint_as_float(r[1]);
}
DEVI float x32_add(float v) {
  u32x2 r = __builtin_amdgcn_permlane32_swap(__float_as_uint(v), __float_as_uint(v), false, false);
  return __uint_as_float(r[0]) + __uint_as_float(r[1]);
}
DEVI float row16_sum(float v) { v = dpp_add<0xB1>(v); v = dpp_add<0x4E>(v); v = dpp_add<0x141>(v); v = dpp_add<0x140>(v); return v; }
DEVI void glds16(const char* g, char* l) {
 __builtin_amdgcn_global_load_lds((const unsigned*)g, (LAS unsigned*)l, 16, 0, 0); }
DEVI unsigned touch(const char* p) { unsigned v; asm volatile("global_load_dword %0, %1, off" : "=v"(v) : "v"(p)); return v; }
DEVI bf16x8 lds_frag(const char* p) { return *(const LAS bf16x8*)p; }
DEVI bf16x8 lds_frag_tr(const char* p, int off2) {
  s16x4 a = __builtin_amdgcn_ds_read_tr16_b64_v4i16((LAS s16x4*)p);
  s16x4 b = __builtin_amdgcn_ds_read_tr16_b64_v4i16((LAS s16x4*)(p + off2));
  return __builtin_bit_cast(bf16x8, __builtin_shufflevector(a, b, 0, 1, 2, 3, 4, 5, 6, 7));
}
DEVI f32x4 mfma(bf16x8 a, bf16x8 b, f32x4 c) {
 return __builtin_amdgcn_mfma_f32_16x16x32_bf16(a, b, c, 0, 0, 0); }
DEVI float mod_at(const float* modp, int i) { return rbf((modp[i] + modp[3456 + i]) + (modp[2 * 3456 + i] + modp[3 * 3456 + i])); }

__global__ __launch_bounds__(256) void k0_ln_mod(const u16* __restrict__ x, const u16* __restrict__ cvec,
                                                 const u16* __restrict__ wmod, const u16* __restrict__ wsre, const u16* __restrict__ wsim,
                                                 const u16* __restrict__ wdre, const u16* __restrict__ wdim, const u16* __restrict__ w1,
                                                 const u16* __restrict__ w2, char* __restrict__ ws) {
  __shared__ __attribute__((aligned(16))) char lds[8 * 1216 + 4 * 64 * 4];
  __shared__ __attribute__((aligned(16))) char pfl[4096];
  const int tid = threadIdx.x, lane = tid & 63, w = __builtin_amdgcn_readfirstlane(tid >> 6), b = blockIdx.x;
  STAMP(0, 0);
  const int tok = 8 * b + 2 * w + (lane >> 5), i = lane & 31;
  const unsigned* x32 = (const unsigned*)x + tok * 288 + i;
  unsigned xr[9];
#pragma unroll
  for (int n = 0; n < 9; n++) xr[n] = x32[32 * n];
  const bool domod = b < 216;
  const int cg = b % 54, kq = b / 54;
  u32x4 wv[5]; u16 cv[5];
  if (domod) {
    const int r0 = tid >> 3, ch = tid & 7;
#pragma unroll
    for (int it = 0; it < 5; it++) {
      int r = r0 + 32 * it; if (r > 143) r = 143;
      r += kq * 144;
      wv[it] = *(const u32x4*)(wmod + (size_t)r * 3456 + cg * 64 + ch * 8);
      cv[it] = cvec[r];
    }
  }
#define PF_MODE 0
  unsigned tv[5];
  {
    const size_t o = (size_t)(b & 7) * (1 << 20) + (b >> 3) * 32768 + tid * 64;
    tv[0] = touch((const char*)wsre + o); tv[1] = touch((const char*)wsim + o);
    tv[2] = touch((const char*)wsre + o + 16384); tv[3] = touch((const char*)wsim + o + 16384);
    int q = (b >> 1) * 81 + (tid < 81 ? tid : 80);
    tv[4] = touch((const char*)((b & 1) ? wdim : wdre) + q * 64);
  }
  float v[18], s = 0.f;
#pragma unroll
  for (int n = 0; n < 9; n++) { v[2 * n] = bf_lo(xr[n]); v[2 * n + 1] = bf_hi(xr[n]); s += v[2 * n] + v[2 * n + 1]; }
  STAMP(0, 1);
  s = x16_add(row16_sum(s));
  const float mean = s * (1.f / D);
  float q = 0.f;
#pragma unroll
  for (int n = 0; n < 18; n++) { v[n] -= mean; q += v[n] * v[n]; }
  q = x16_add(row16_sum(q));
  const float rs = rsqrtf(q * (1.f / D) + 1e-5f);
  {
    char* row = lds + (2 * w + (lane >> 5)) * 1216 + i * 4;
#pragma unroll
    for (int n = 0; n < 9; n++) *(LAS unsigned*)(row + 128 * n) = pk2(v[2 * n] * rs, v[2 * n + 1] * rs);
  }
  float* red = (float*)(lds + 8 * 1216);
  if (domod) {
    float a[8] = {0, 0, 0, 0, 0, 0, 0, 0};
#pragma unroll
    for (int it = 0; it < 5; it++) {
      float c = bf_lo(cv[it]);
      float sl = rbf(c / (1.f + __expf(-c)));
      if (it == 4 && (tid >> 3) >= 16) sl = 0.f;
#pragma unroll
      for (int e = 0; e < 4; e++) { a[2 * e] += sl * bf_lo(wv[it][e]); a[2 * e + 1] += sl * bf_hi(wv[it][e]); }
    }
#pragma unroll
    for (int e = 0; e < 8; e++) a[e] = x32_add(x16_add(dpp_add<0x128>(a[e])));
    if (lane < 8) {
      *(LAS f32x4*)(red + w * 64 + lane * 8) = f32x4{a[0], a[1], a[2], a[3]};
      *(LAS f32x4*)(red + w * 64 + lane * 8 + 4) = f32x4{a[4], a[5], a[6], a[7]};
    }
  }
  __syncthreads();
  STAMP(0, 2);
  if (domod && tid < 64) {
    float* modp = (float*)(ws + WS_MODP);
    modp[kq * 3456 + cg * 64 + tid] = (red[tid] + red[64 + tid]) + (red[128 + tid] + red[192 + tid]);
  }
  const int grp = lane >> 4, c = lane & 15;
  for (int it = w; it < 9; it += 4) {
    const char* p = lds + (c >> 2) * 1216 + (64 * it + 16 * grp + 4 * (c & 3)) * 2;
    bf16x8 f = lds_frag_tr(p, 4 * 1216);
    const int J = 4 * it + grp, nb = J / 9, j = J - nb * 9;
    char* dst = ws + WS_XNT + ((size_t)((nb * 64 + (b >> 2)) * 9 + j) << 10) + (((b & 3) * 16 + c) << 4);
    *(bf16x8*)dst = f;
  }
  WAIT_V(0);
#pragma unroll
  for (int i = 0; i < 5; i++) asm volatile("" ::"v"(tv[i]));
  STAMP(0, 3);
}

constexpr int NL = 2;
constexpr int NT = 256 + 64 * NL;
#define K1_MAP 0
constexpr int K1_SLOT = 34816, K1_NS = 3, K1_NST = 16;
__global__ __launch_bounds__(NT) void k1_seq(const u16* __restrict__ wsre, const u16* __restrict__ wsim, const u16* __restrict__ w1,
                                             const u16* __restrict__ w2, char* __restrict__ ws) {
  __shared__ __attribute__((aligned(16))) char lds[K1_NS * K1_SLOT + 1024 + 144 * 8 + 512 + 16];
  char* const ones = lds + K1_NS * K1_SLOT;
  float* const sca = (float*)(ones + 1024);
  float* const ssa = sca + 144;
  float* const srs = ssa + 144;
  volatile LAS int* const sflag = (volatile LAS int*)(srs + 128);
  const int tid = threadIdx.x, lane = tid & 63, w = __builtin_amdgcn_readfirstlane(tid >> 6);
  const int xcd = blockIdx.x & 7, idx = blockIdx.x >> 3;
  int ks, nb, mb;
  STAMP(1, 0);
  if (K1_MAP == 0) { ks = idx & 1; nb = (idx >> 1) & 3; const int ms = idx >> 3; mb = (ms >> 1) * 16 + 2 * xcd + (ms & 1); }
  else { ks = xcd & 1; nb = idx & 3; const int ms = idx >> 2; mb = (ms >> 2) * 16 + 4 * (xcd >> 1) + (ms & 3); }
  if (w >= 4) {
    const int li = w - 4;
    const char* wsb = (const char*)(mb >= 16 ? wsim : wsre) + (size_t)(mb & 15) * 128 * 4096 + ks * 2048;
    const char* xb = ws + WS_XNT + (size_t)(nb * 64 + ks * 32) * 9216;
    const unsigned vw = (lane >> 3) * 4096 + (((lane & 7) ^ (lane >> 3)) << 4), vl = lane << 4;
    auto issue = [&](int s) {
      char* slot = lds + (s % K1_NS) * K1_SLOT;
      if (li == 0) {
#pragma unroll
        for (int p = 0; p < 16; p++) glds16(wsb + s * 128 + p * 32768 + vw, slot + p * 1024);
        glds16(xb + s * 18432 + vl, slot + 16384);
      } else {
#pragma unroll
        for (int p = 1; p < 18; p++) glds16(xb + s * 18432 + p * 1024 + vl, slot + 16384 + p * 1024);
      }
    };
    LTDECL; LT(0);
    issue(0); LT(1); issue(1); LT(2);
    WAIT_V(17);
    LT(3);
    BAR();
    for (int s = 2; s < K1_NST; s++) { issue(s); WAIT_V(17); BAR(); }
    WAIT_V(0); BAR();
    {
      const int b = blockIdx.x, th = b / 72, cg = b - th * 72, r0 = th * 192 + li * 96;
      unsigned tv[2];
      const char* tp = (const char*)w2 + (size_t)(b * 2 + li) * 10368;
      tv[0] = touch(tp + lane * 128); tv[1] = touch(tp + 8192 + (lane < 17 ? lane : 16) * 128);
      if (b < 216) {
        const float* modp = (const float*)(ws + WS_MODP) + 3 * 576 + r0;
        float mq[2][8];
#pragma unroll
        for (int r = 0; r < 2; r++)
#pragma unroll
          for (int i = 0; i < 8; i++) mq[r][i] = modp[(i >> 1) * 3456 + (i & 1) * 576 + (r ? 64 + (lane & 31) : lane)];
        u32x4 wv[12];
#pragma unroll
        for (int it = 0; it < 12; it++) wv[it] = *(const u32x4*)(w1 + (size_t)(r0 + (lane >> 3) + 8 * it) * F + cg * 64 + (lane & 7) * 8);
        SCHED();
        int kb[2], kc[2];
#pragma unroll
        for (int r = 0; r < 2; r++) {
          kb[r] = __float_as_int(rbf((mq[r][0] + mq[r][2]) + (mq[r][4] + mq[r][6])));
          kc[r] = __float_as_int(rbf(1.f + rbf((mq[r][1] + mq[r][3]) + (mq[r][5] + mq[r][7]))));
        }
        float u[8] = {0, 0, 0, 0, 0, 0, 0, 0}, v[8] = {0, 0, 0, 0, 0, 0, 0, 0};
#pragma unroll
        for (int it = 0; it < 12; it++) {
          const int sl = ((lane >> 3) + 8 * (it & 7)) << 2;
          const float a = __int_as_float(__builtin_amdgcn_ds_bpermute(sl, kc[it >> 3])), bb = __int_as_float(__builtin_amdgcn_ds_bpermute(sl, kb[it >> 3]));
#pragma unroll
          for (int e = 0; e < 4; e++) {
            const float lo = bf_lo(wv[it][e]), hi = bf_hi(wv[it][e]);
            u[2 * e] += a * lo; u[2 * e + 1] += a * hi; v[2 * e] += bb * lo; v[2 * e + 1] += bb * hi;
          }
        }
#pragma unroll
        for (int e = 0; e < 8; e++) { u[e] = x32_add(x16_add(dpp_add<0x128>(u[e]))); v[e] = x32_add(x16_add(dpp_add<0x128>(v[e]))); }
        if (lane < 8) {
          float* r = (float*)(ws + WS_UVP) + (th * 2 + li) * 2 * F + cg * 64 + lane * 8;
          *(f32x4*)(r) = f32x4{u[0], u[1], u[2], u[3]}; *(f32x4*)(r + 4) = f32x4{u[4], u[5], u[6], u[7]};
          *(f32x4*)(r + F) = f32x4{v[0], v[1], v[2], v[3]}; *(f32x4*)(r + F + 4) = f32x4{v[4], v[5], v[6], v[7]};
        }
      }
      WAIT_V(0);
      asm volatile("" ::"v"(tv[0]), "v"(tv[1]));
    }
    LTOUT(6);
    return;
  }
  const int mh = w >> 1, nh = w & 1, g = lane >> 4, c = lane & 15;
  float mq[8];
  {
    const float* modp = (const float*)(ws + WS_MODP) + nb * 144 + (tid < 144 ? tid : 0);
#pragma unroll
    for (int i = 0; i < 8; i++) mq[i] = modp[(i >> 1) * 3456 + (i & 1) * 576];
    *(LAS unsigned*)(ones + tid * 4) = 0x3f803f80u;
    if (tid < 2) sflag[tid] = 0;
  }
  f32x4 acc[4][5];
#pragma unroll
  for (int m = 0; m < 4; m++)
#pragma unroll
    for (int j = 0; j < 5; j++) { acc[m][j] = f32x4{0, 0, 0, 0}; PIN(acc[m][j]); }
  const int vb = (64 * mh + c) * 128 + ((g ^ (c & 7)) << 4);
  const int va = 16384 + nh * 5120 + (lane << 4);
  bf16x8 B0[4], A0[5], B1[4], A1[5];
  auto rd = [&](bf16x8* B, bf16x8* A, int s, int hk) {
    const char* slot = lds + (s % K1_NS) * K1_SLOT;
    const char* pb = slot + (vb ^ (hk << 6));
    const char* pa = slot + va + hk * 9216;
    const char* p4 = nh ? ones + (lane << 4) : pa + 4096;
    B[0] = lds_frag(pb); A[0] = lds_frag(pa);
#pragma unroll
    for (int m = 1; m < 4; m++) B[m] = lds_frag(pb + m * 2048);
#pragma unroll
    for (int j = 1; j < 4; j++) A[j] = lds_frag(pa + j * 1024);
    A[4] = lds_frag(p4);
  };
  auto mm = [&](bf16x8* B, bf16x8* A) {
#pragma unroll
    for (int j = 0; j < 5; j++)
#pragma unroll
      for (int m = 0; m < 4; m++) acc[m][j] = mfma(A[j], B[m], acc[m][j]);
  };
  auto pat = [&]() {
#pragma unroll
    for (int i = 0; i < 9; i++) { SGB(0x008, 1); SGB(0x100, 1); }
    SGB(0x008, 11);
  };
  WAIT_L0();
  BAR();
  STAMP(1, 1);
  rd(B0, A0, 0, 0);
  if (tid < 144) { ssa[tid] = rbf((mq[0] + mq[2]) + (mq[4] + mq[6])); sca[tid] = rbf(1.f + rbf((mq[1] + mq[3]) + (mq[5] + mq[7]))); }
  for (int s = 0; s < K1_NST; s++) {
    SCHED();
    rd(B1, A1, s, 1); mm(B0, A0); pat();
    SCHED();
    if (s + 1 < K1_NST) {
      WAIT_L0(); BAR();
      SCHED();
      rd(B0, A0, s + 1, 0); mm(B1, A1); pat();
    } else {
      mm(B1, A1);
    }
  }
  SCHED();
  STAMP(1, 2);
  if (nh == 1) {
    if (g == 0) {
#pragma unroll
      for (int m = 0; m < 4; m++) srs[64 * mh + 16 * m + c] = acc[m][4][0];
    }
    WAIT_L0();
    if (lane == 0) sflag[mh] = 1;
  } else {
    while (sflag[mh] == 0) __builtin_amdgcn_s_sleep(1);
  }
  asm volatile("" ::: "memory");
  float rsum[4];
#pragma unroll
  for (int m = 0; m < 4; m++) rsum[m] = srs[64 * mh + 16 * m + c];
  const float sgn = mb >= 16 ? -1.f : 1.f;
  const int tb = (mb & 15) * 4 + 2 * mh;
  char* const pq = ws + WS_PQ;
#pragma unroll
  for (int j = 0; j < 5; j++) {
    if (j == 4 && nh) break;
    const int jj = 5 * nh + j;
    f32x4 ca = *(const LAS f32x4*)(sca + 16 * jj + 4 * g), sa = *(const LAS f32x4*)(ssa + 16 * jj + 4 * g);
    ca *= sgn; sa *= sgn;
    const int Jp = (mb >> 4) * 36 + 9 * nb + jj;
#pragma unroll
    for (int mp = 0; mp < 2; mp++) {
      f32x4 p0 = acc[2 * mp][j] * ca + rsum[2 * mp] * sa, p1 = acc[2 * mp + 1][j] * ca + rsum[2 * mp + 1] * sa;
      u32x2 lo = __builtin_amdgcn_permlane16_swap(pk2(p0[0], p0[1]), pk2(p1[0], p1[1]), false, false);
      u32x2 hi = __builtin_amdgcn_permlane16_swap(pk2(p0[2], p0[3]), pk2(p1[2], p1[3]), false, false);
      u32x4 o = {lo[0], hi[0], lo[1], hi[1]};
      const unsigned off = (unsigned)(((((tb + mp) * 36 + (Jp >> 1)) * 2 + ks) * 2 + (g & 1)) << 10) + ((((Jp & 1) * 2 + (g >> 1)) * 16 + c) << 4);
      *(u32x4*)(pq + off) = o;
    }
  }
  STAMP(1, 3);
}

#define K2_D 3
#define K2_NOTCH
constexpr int K2_SLOT = 26624, K2_NS = K2_D + 1, K2_NST = 18;
__global__ __launch_bounds__(NT) void k2_hid(const u16* __restrict__ x, const u16* __restrict__ wdre, const u16* __restrict__ wdim,
                                             char* __restrict__ ws) {
  __shared__ __attribute__((aligned(16))) char lds[K2_NS * K2_SLOT + 144 * 2 * 4 + 256];
  float* const sga = (float*)(lds + K2_NS * K2_SLOT);
  float* const scb = sga + 144;
  const int tid = threadIdx.x, lane = tid & 63, w = __builtin_amdgcn_readfirstlane(tid >> 6);
  const int xcd = blockIdx.x & 7, idx = blockIdx.x >> 3;
  const int nb = idx & 3, tb = xcd * 8 + (idx >> 2);
  STAMP(2, 0);
  if (w >= 4) {
    const int li = w - 4;
    const unsigned vw = (lane >> 3) * 1152 + (((lane & 7) ^ (lane >> 3)) << 4), vl = lane << 4;
    const char* pb = ws + WS_PQ + (size_t)tb * 36 * 4096;
    auto issue = [&](int s) {
      char* slot = lds + (s % K2_NS) * K2_SLOT;
      const char* wb = (const char*)(s >= 9 ? wdim : wdre) + nb * 144 * 1152 + (s >= 9 ? s - 9 : s) * 128;
      if (li == 0) {
#pragma unroll
        for (int p = 0; p < 13; p++) glds16(wb + p * 9216 + vw, slot + p * 1024);
      } else {
#pragma unroll
        for (int p = 0; p < 8; p++) glds16(pb + s * 8192 + p * 1024 + vl, slot + 18432 + p * 1024);
#pragma unroll
        for (int p = 13; p < 18; p++) glds16(wb + p * 9216 + vw, slot + p * 1024);
      }
    };
    const int tl = li ? (lane < 48 ? lane : 47) : (lane < 54 ? lane : 53);
    const int ts = li ? tl >> 4 : tl / 18;
    const unsigned to = li ? ts * 8192 + (nb * 16 + (tl & 15)) * 128 : ts * 128 + (nb * 144 + (idx >> 2) * 18 + tl - ts * 18) * 1152;
    auto tch = [&](int G) {
    };
    constexpr int T1 = 0;
    tch(0); tch(1);
    issue(0); issue(1);
    if (K2_D == 2) {
      WAIT_V(13); BAR();
      for (int s = 2; s < 14; s += 3) {
        tch((s + 4) / 3); issue(s); WAIT_V(13 + T1); BAR();
        issue(s + 1); WAIT_V(13); BAR();
        issue(s + 2); WAIT_V(13); BAR();
      }
      for (int s = 14; s < K2_NST; s++) { issue(s); WAIT_V(13); BAR(); }
      WAIT_V(0); BAR();
    } else {
      tch(2); issue(2);
      WAIT_V(26 + T1); BAR();
      issue(3); WAIT_V(26 + T1); BAR();
      issue(4); WAIT_V(26); BAR();
      for (int s = 5; s < 14; s += 3) {
        tch((s + 4) / 3); issue(s); WAIT_V(26 + T1); BAR();
        issue(s + 1); WAIT_V(26 + T1); BAR();
        issue(s + 2); WAIT_V(26); BAR();
      }
      for (int s = 14; s < K2_NST; s++) { issue(s); WAIT_V(26); BAR(); }
      WAIT_V(13); BAR(); WAIT_V(0); BAR();
    }
    f32x4 q[6];
    const int qi = blockIdx.x * 9 + (lane < 9 ? lane : 8);
    if (li == 0) {
#pragma unroll
      for (int i = 0; i < 6; i++) q[i] = ((const f32x4*)(ws + WS_UVP))[i * (2 * F / 4) + qi];
    }
    BAR(); BAR();
    if (li == 0 && lane < 9) ((f32x4*)(ws + WS_UV))[qi] = ((q[0] + q[1]) + (q[2] + q[3])) + (q[4] + q[5]);
    return;
  }
  const int mh = w >> 1, kh = w & 1, g = lane >> 4, c = lane & 15;
  u32x2 xr[5];
  const int tok = tb * 32 + mh * 16 + c;
#pragma unroll
  for (int jj = 0; jj < 5; jj++) {
    int j = kh * 5 + jj; if (j > 8) j = 8;
    xr[jj] = *(const u32x2*)(x + (size_t)tok * D + nb * 144 + 16 * j + 4 * g);
  }
  float mq[8];
  {
    const float* modp = (const float*)(ws + WS_MODP) + 2 * 576 + nb * 144 + (tid < 144 ? tid : 0);
#pragma unroll
    for (int i = 0; i < 8; i++) mq[i] = modp[(i >> 1) * 3456 + (i & 1) * 1152];
  }
  f32x4 acc[9];
#pragma unroll
  for (int j = 0; j < 9; j++) { acc[j] = f32x4{0, 0, 0, 0}; PIN(acc[j]); }
  const int va = c * 128 + (((4 * kh + g) ^ (c & 7)) << 4);
  const int vb = 18432 + kh * 4096 + mh * 1024 + (lane << 4);
  bf16x8 Bx[2], By[2], A0[5], A1[4];
  auto rd0 = [&](bf16x8* B, int s) {
    const char* slot = lds + (s % K2_NS) * K2_SLOT;
    B[0] = lds_frag(slot + vb); A0[0] = lds_frag(slot + va); B[1] = lds_frag(slot + vb + 2048);
#pragma unroll
    for (int j = 1; j < 5; j++) A0[j] = lds_frag(slot + va + j * 2048);
  };
  auto rd1 = [&](int s) {
    const char* slot = lds + (s % K2_NS) * K2_SLOT;
#pragma unroll
    for (int j = 0; j < 4; j++) A1[j] = lds_frag(slot + va + (5 + j) * 2048);
  };
  auto mm0 = [&](bf16x8* B) {
#pragma unroll
    for (int j = 0; j < 5; j++) { acc[j] = mfma(A0[j], B[0], acc[j]); }
#pragma unroll
    for (int j = 0; j < 5; j++) { acc[j] = mfma(A0[j], B[1], acc[j]); }
  };
  auto mm1 = [&](bf16x8* B) {
#pragma unroll
    for (int j = 0; j < 4; j++) { acc[5 + j] = mfma(A1[j], B[0], acc[5 + j]); }
#pragma unroll
    for (int j = 0; j < 4; j++) { acc[5 + j] = mfma(A1[j], B[1], acc[5 + j]); }
  };
  auto step = [&](bf16x8* Bc, bf16x8* Bn, int s, bool last) {
    SCHED();
    rd1(s); mm0(Bc);
#pragma unroll
    for (int i = 0; i < 4; i++) { SGB(0x008, 1); SGB(0x100, 1); }
    SGB(0x008, 6);
    SCHED();
    if (!last) {
      WAIT_L0(); BAR();
      SCHED();
      rd0(Bn, s + 1); mm1(Bc);
#pragma unroll
      for (int i = 0; i < 7; i++) { SGB(0x008, 1); SGB(0x100, 1); }
      SGB(0x008, 1);
    } else mm1(Bc);
  };
  BAR();
  STAMP(2, 1);
  rd0(Bx, 0);
  for (int s = 0; s < K2_NST - 2; s += 2) { step(Bx, By, s, false); step(By, Bx, s + 1, false); }
  step(Bx, By, K2_NST - 2, false); step(By, Bx, K2_NST - 1, true);
  SCHED();
  STAMP(2, 2);
  if (tid < 144) { sga[tid] = rbf((mq[0] + mq[2]) + (mq[4] + mq[6])); scb[tid] = rbf(1.f + rbf((mq[1] + mq[3]) + (mq[5] + mq[7]))); }
  WAIT_L0(); BAR();
  {
    char* xb = lds + mh * 9216 + (lane << 4);
    if (kh == 0) {
#pragma unroll
      for (int j = 5; j < 9; j++) *(LAS f32x4*)(xb + j * 1024) = acc[j];
    } else {
#pragma unroll
      for (int j = 0; j < 5; j++) *(LAS f32x4*)(xb + j * 1024) = acc[j];
    }
    WAIT_L0(); BAR();
    if (kh == 0) {
#pragma unroll
      for (int j = 0; j < 5; j++) acc[j] += *(const LAS f32x4*)(xb + j * 1024);
    } else {
#pragma unroll
      for (int j = 0; j < 4; j++) acc[j] = acc[5 + j] + *(const LAS f32x4*)(xb + (5 + j) * 1024);
    }
  }
  const int nj = 5 - kh;
  float sum = 0.f;
#pragma unroll
  for (int jj = 0; jj < 5; jj++) {
    if (jj < nj) {
      const int j = kh * 5 + jj;
      f32x4 ga = *(const LAS f32x4*)(sga + 16 * j + 4 * g), cb = *(const LAS f32x4*)(scb + 16 * j + 4 * g);
      f32x4 xv = {bf_lo(xr[jj][0]), bf_hi(xr[jj][0]), bf_lo(xr[jj][1]), bf_hi(xr[jj][1])};
      f32x4 x2 = xv + ga * acc[jj];
      acc[jj] = x2;
      sum += (x2[0] + x2[1]) + (x2[2] + x2[3]);
      u32x2 o = {pk2(x2[0], x2[1]), pk2(x2[2], x2[3])};
      *(u32x2*)(ws + WS_X2 + (((((tb * 4 + nb) * 9 + j) * 2 + mh) * 64 + lane) << 3)) = o;
      f32x4 xs = x2 * cb;
      u32x2 o2 = {pk2(xs[0], xs[1]), pk2(xs[2], xs[3])};
      const int J = 9 * nb + j;
      *(u32x2*)(ws + WS_X2P + ((((tb >> 1) * 18 + (J >> 1)) * 4 + (tb & 1) * 2 + mh) << 10) + (lane << 4) + (J & 1) * 8) = o2;
    }
  }
  sum = x32_add(x16_add(sum));
  const float lm = sum * (kh ? 1.f / 64 : 1.f / 80);
  float m2 = 0.f;
#pragma unroll
  for (int jj = 0; jj < 5; jj++) {
    if (jj < nj) {
      f32x4 d = acc[jj] - lm;
      m2 += (d[0] * d[0] + d[1] * d[1]) + (d[2] * d[2] + d[3] * d[3]);
    }
  }
  m2 = x32_add(x16_add(m2));
  if (g == 0) *(f32x2*)(ws + WS_STAT + (((nb * 2 + kh) * S + tok) << 3)) = f32x2{sum, m2};
  STAMP(2, 3);
}

constexpr int KM_ACT = 73728, KM_SLOT = 20480;
__global__ __launch_bounds__(NT) void km_mlp(const u16* __restrict__ w1, const u16* __restrict__ w2, char* __restrict__ ws) {
  __shared__ __attribute__((aligned(16))) char lds[KM_ACT + 4 * KM_SLOT + 256];
  char* const ring = lds + KM_ACT;
  const int tid = threadIdx.x, lane = tid & 63, w = __builtin_amdgcn_readfirstlane(tid >> 6);
  const int fs = blockIdx.x & 7, tt = blockIdx.x >> 3;
  STAMP(3, 0);
  if (w >= 4) {
    const int li = w - 4;
    const unsigned sw = ((lane & 7) ^ (((lane >> 4) & 3) << 1)) << 4;
    const unsigned v1 = (lane >> 3) * 9216 + sw, v2 = (lane >> 3) * 1152 + sw, vl = lane << 4;
    const char* xb = ws + WS_X2P + (size_t)tt * KM_ACT + li * 2048;
    const char* w1b = (const char*)w1 + fs * 1152 + li * 16 * 9216;
    const char* w2b = (const char*)w2 + (size_t)fs * 576 * 1152;
    char* const rl = ring + li * 2048;
    auto is1 = [&](int s, int h) {
      char* slot = rl + ((s & 1) * 2 + h) * KM_SLOT;
      const char* src = w1b + (size_t)s * 32 * 9216 + h * 640;
#pragma unroll
      for (int j = 0; j < 5; j++) {
        if (j < 5 - h) { glds16(src + j * 128 + v1, slot + j * 4096); glds16(src + j * 128 + 8 * 9216 + v1, slot + j * 4096 + 1024); }
      }
    };
    auto is2 = [&](int kb, int h) {
      char* slot = rl + ((kb & 1) * 2 + h) * KM_SLOT;
      const int row = kb < 16 ? 128 * (kb >> 2) + 64 * li + 16 * (kb & 3) : 32 * kb + 16 * li;
      const char* src = w2b + row * 1152 + h * 640;
#pragma unroll
      for (int j = 0; j < 5; j++) {
        if (j < 5 - h) { glds16(src + j * 128 + v2, slot + j * 4096); glds16(src + j * 128 + 8 * 1152 + v2, slot + j * 4096 + 1024); }
      }
    };
    auto isx = [&](int s) { glds16(xb + s * 4096 + vl, lds + s * 4096 + li * 2048); glds16(xb + s * 4096 + 1024 + vl, lds + s * 4096 + li * 2048 + 1024); };
    const int tl = lane < 63 ? lane : 62, tr = tl / 9, tc = tl - 9 * tr;
    auto tch = [&](int r) {
      int row = 224 * r + 7 * tt + tr; if (row > 1151) row = 1151;
      const char* a = row < 576 ? (const char*)w1 + fs * 1152 + (size_t)row * 9216 : w2b + (row - 576) * 1152;
      if (li == 1) __builtin_amdgcn_global_load_lds((const unsigned*)(a + tc * 128), (LAS unsigned*)(ring + 4 * KM_SLOT), 4, 0, 0);
    };
    auto isx4 = [&](int s) { isx(s); isx(s + 1); isx(s + 2); isx(s + 3); };
    LTDECL; LT(0);
    tch(0);
    isx(0); is1(0, 0); LT(1); is1(0, 1); isx(1); is1(1, 0);
    LT(2);
    WAIT_V(20);
    LT(3);
    BAR();
    is1(1, 1); isx4(2); tch(1);
    WAIT_V(28); BAR(); is1(2, 0); WAIT_V(26); BAR(); is1(2, 1);
    WAIT_V(26); BAR(); is1(3, 0); WAIT_V(18); BAR(); is1(3, 1);
    WAIT_V(18); BAR(); is1(4, 0); WAIT_V(18); BAR(); is1(4, 1);
    for (int s = 5; s < 17; s += 4) {
      WAIT_V(18); BAR(); is1(s, 0); isx4(s + 1); tch((s + 3) >> 2); if (s == 13) tch(5);
      WAIT_V(26); BAR(); is1(s, 1);
      WAIT_V(26); BAR(); is1(s + 1, 0); WAIT_V(26); BAR(); is1(s + 1, 1);
      WAIT_V(18); BAR(); is1(s + 2, 0); WAIT_V(18); BAR(); is1(s + 2, 1);
      WAIT_V(18); BAR(); is1(s + 3, 0); WAIT_V(18); BAR(); is1(s + 3, 1);
    }
    WAIT_V(18); BAR(); is1(17, 0); WAIT_V(18); BAR(); is1(17, 1);
    for (int kb = 0; kb < 18; kb++) { WAIT_V(18); BAR(); is2(kb, 0); WAIT_V(18); BAR(); is2(kb, 1); }
    WAIT_V(18); BAR(); WAIT_V(8); BAR(); WAIT_V(0); BAR();
    LTOUT(7);
    return;
  }
  const int g = lane >> 4, c = lane & 15;
  f32x4 acc[4][9];
  {
    f32x2 p[8]; f32x4 u[9], v[9];
    const f32x2* st = (const f32x2*)(ws + WS_STAT) + tt * 64 + lane;
#pragma unroll
    for (int i = 0; i < 8; i++) p[i] = st[i * S];
    const f32x4* uv = (const f32x4*)(ws + WS_UV) + fs * 144 + 4 * w + g;
#pragma unroll
    for (int j = 0; j < 9; j++) { u[j] = uv[16 * j]; v[j] = uv[F / 4 + 16 * j]; }
    SCHED();
    float sum = 0.f;
#pragma unroll
    for (int i = 0; i < 8; i++) sum += p[i][0];
    const float mean = sum * (1.f / D);
    float m2 = 0.f;
#pragma unroll
    for (int i = 0; i < 8; i++) {
      const float n = (i & 1) ? 64.f : 80.f, d = p[i][0] * ((i & 1) ? 1.f / 64 : 1.f / 80) - mean;
      m2 += p[i][1] + n * d * d;
    }
    const float var = m2 * (1.f / D) + 1e-5f, rs = rsqrtf(var);
    if (fs == 0 && w == 0) ((float*)(ws + WS_RSTD))[tt * 64 + lane] = rs;
    float nm[4], sd[4];
#pragma unroll
    for (int m = 0; m < 4; m++) {
      nm[m] = __int_as_float(__builtin_amdgcn_ds_bpermute((16 * m + c) << 2, __float_as_int(-mean)));
      sd[m] = __int_as_float(__builtin_amdgcn_ds_bpermute((16 * m + c) << 2, __float_as_int(var * rs)));
    }
#pragma unroll
    for (int j = 0; j < 9; j++) {
#pragma unroll
      for (int m = 0; m < 4; m++) { acc[m][j] = sd[m] * v[j] + nm[m] * u[j]; PIN(acc[m][j]); }
    }
  }
  const int va = KM_ACT + (4 * g + (c >> 2)) * 128 + ((w ^ ((2 * g + (c >> 3)) & 3)) << 5) + (c & 3) * 8;
  const int vb = lane << 4;
  bf16x8 Bx[4], By[4], A0[5], A1[4];
  auto rdA0 = [&](int par) {
#pragma unroll
    for (int j = 0; j < 5; j++) A0[j] = lds_frag_tr(lds + va + par * 2 * KM_SLOT + j * 4096, 2048);
  };
  auto rdA1 = [&](int par) {
#pragma unroll
    for (int j = 0; j < 4; j++) A1[j] = lds_frag_tr(lds + va + (par * 2 + 1) * KM_SLOT + j * 4096, 2048);
  };
  auto rdB = [&](bf16x8* B, int kb) {
#pragma unroll
    for (int m = 0; m < 4; m++) B[m] = lds_frag(lds + vb + kb * 4096 + m * 1024);
  };
  auto step = [&](bf16x8* Bc, bf16x8* Bn, int kb, int par, int mode) {
    const f32x4 zero = {0, 0, 0, 0};
    WAIT_L0(); BAR();
    SCHED();
    if (mode == 1) { rdB(Bc, kb); SCHED(); }
    rdA1(par);
#pragma unroll
    for (int j = 0; j < 5; j++)
#pragma unroll
      for (int m = 0; m < 4; m++) acc[m][j] = mfma(A0[j], Bc[m], mode == 1 ? zero : acc[m][j]);
#pragma unroll
    for (int i = 0; i < 8; i++) { SGB(0x008, 1); SGB(0x100, 1); }
    SGB(0x008, 12);
    SCHED();
    if (mode != 3) {
      WAIT_L0(); BAR();
      SCHED();
      rdA0(par ^ 1);
      if (mode != 2) rdB(Bn, kb + 1);
    }
#pragma unroll
    for (int j = 0; j < 4; j++)
#pragma unroll
      for (int m = 0; m < 4; m++) acc[m][5 + j] = mfma(A1[j], Bc[m], mode == 1 ? zero : acc[m][5 + j]);
    if (mode < 2) {
#pragma unroll
      for (int i = 0; i < 7; i++) { SGB(0x008, 1); SGB(0x100, 2); }
      SGB(0x008, 9);
    } else if (mode == 2) {
#pragma unroll
      for (int i = 0; i < 5; i++) { SGB(0x008, 1); SGB(0x100, 2); }
      SGB(0x008, 11);
    }
    SCHED();
  };
  BAR();
  STAMP(3, 1);
  rdA0(0); rdB(Bx, 0);
  for (int kb = 0; kb < 18; kb += 2) { step(Bx, By, kb, 0, 0); step(By, Bx, kb + 1, 1, 0); }
  STAMP(3, 2);
  {
    auto rp = [&](f32x4 a) {
      typedef short s16x2 __attribute__((ext_vector_type(2)));
      const s16x2 z = {0, 0};
      unsigned p0 = pk2(a[0], a[1]), p1 = pk2(a[2], a[3]);
      asm("" : "+v"(p0)); asm("" : "+v"(p1));
      s16x2 lo = __builtin_elementwise_max(__builtin_bit_cast(s16x2, p0), z), hi = __builtin_elementwise_max(__builtin_bit_cast(s16x2, p1), z);
      return u32x2{__builtin_bit_cast(unsigned, lo), __builtin_bit_cast(unsigned, hi)};
    };
    char* const hb = lds + vb + w * 4096;
#pragma unroll
    for (int p = 0; p < 4; p++)
#pragma unroll
      for (int m = 0; m < 4; m++) {
        const u32x2 a = rp(acc[m][2 * p]), b = rp(acc[m][2 * p + 1]);
        *(LAS u32x4*)(hb + p * 16384 + m * 1024) = u32x4{a[0], a[1], b[0], b[1]};
      }
    char* const h8 = lds + vb + (16 + (w >> 1)) * 4096 + (w & 1) * 8;
#pragma unroll
    for (int m = 0; m < 4; m++) *(LAS u32x2*)(h8 + m * 1024) = rp(acc[m][8]);
  }
  SCHED();
  STAMP(4, 0);
  step(Bx, By, 0, 0, 1);
  STAMP(4, 1);
  for (int kb = 1; kb < 17; kb += 2) { step(By, Bx, kb, 1, 0); step(Bx, By, kb + 1, 0, 0); }
  step(By, Bx, 17, 1, 3);
  STAMP(4, 2);
  char* const pb = ws + WS_PART + ((size_t)((tt * 72 + w) * 8 + fs) << 10) + (lane << 4);
#pragma unroll
  for (int j = 0; j < 9; j++)
#pragma unroll
    for (int mp = 0; mp < 2; mp++) {
      const f32x4 a = acc[2 * mp][j], b = acc[2 * mp + 1][j];
      *(u32x4*)(pb + (mp * 36 + 4 * j) * 8192) = u32x4{pk2(a[0], a[1]), pk2(a[2], a[3]), pk2(b[0], b[1]), pk2(b[2], b[3])};
    }
  STAMP(3, 3);
  STAMP(4, 3);
}

__global__ __launch_bounds__(576) void k5_out(char* __restrict__ ws, u16* __restrict__ out) {
  const int lane = threadIdx.x & 63, j = threadIdx.x >> 6, g = lane >> 4, c = lane & 15;
  const int xcd = blockIdx.x & 7, idx = blockIdx.x >> 3;
  const int nb = idx & 3, tb = xcd * 8 + (idx >> 2);
  STAMP(5, 0);
  const int u = (tb * 4 + nb) * 9 + j;
  const char* pp = ws + WS_PART + ((size_t)u << 13) + (lane << 4);
  u32x4 p[8];
#pragma unroll
  for (int i = 0; i < 8; i++) p[i] = *(const u32x4*)(pp + i * 1024);
  const float ra = ((const float*)(ws + WS_RSTD))[tb * 32 + c], rb = ((const float*)(ws + WS_RSTD))[tb * 32 + 16 + c];
  u32x2 xa = *(const u32x2*)(ws + WS_X2 + ((size_t)u << 10) + (lane << 3)), xb = *(const u32x2*)(ws + WS_X2 + ((size_t)u << 10) + 512 + (lane << 3));
  const int n = nb * 144 + 16 * j + 4 * g;
  const float* mp = (const float*)(ws + WS_MODP) + 5 * 576 + n;
  f32x4 gb = (*(const f32x4*)mp + *(const f32x4*)(mp + 3456)) + (*(const f32x4*)(mp + 2 * 3456) + *(const f32x4*)(mp + 3 * 3456));
#pragma unroll
  for (int e = 0; e < 4; e++) gb[e] = rbf(gb[e]);
  STAMP(5, 1);
  float z[8];
#pragma unroll
  for (int e = 0; e < 4; e++) {
    const float r = e < 2 ? ra : rb;
    z[2 * e] = r * (((bf_lo(p[0][e]) + bf_lo(p[1][e])) + (bf_lo(p[2][e]) + bf_lo(p[3][e]))) + ((bf_lo(p[4][e]) + bf_lo(p[5][e])) + (bf_lo(p[6][e]) + bf_lo(p[7][e]))));
    z[2 * e + 1] = r * (((bf_hi(p[0][e]) + bf_hi(p[1][e])) + (bf_hi(p[2][e]) + bf_hi(p[3][e]))) + ((bf_hi(p[4][e]) + bf_hi(p[5][e])) + (bf_hi(p[6][e]) + bf_hi(p[7][e]))));
  }
  u32x2 oa = {pk2(bf_lo(xa[0]) + gb[0] * z[0], bf_hi(xa[0]) + gb[1] * z[1]), pk2(bf_lo(xa[1]) + gb[2] * z[2], bf_hi(xa[1]) + gb[3] * z[3])};
  u32x2 ob = {pk2(bf_lo(xb[0]) + gb[0] * z[4], bf_hi(xb[0]) + gb[1] * z[5]), pk2(bf_lo(xb[1]) + gb[2] * z[6], bf_hi(xb[1]) + gb[3] * z[7])};
  STAMP(5, 2);
  *(u32x2*)(out + (size_t)(tb * 32 + c) * D + n) = oa;
  *(u32x2*)(out + (size_t)(tb * 32 + 16 + c) * D + n) = ob;
  STAMP(5, 3);
}

extern "C" void kernel_launch(void* const* d_in, const int* in_sizes, int n_in, void* d_out, int out_size, void* d_ws,
                              size_t ws_size, hipStream_t stream) {
  const u16* x = (const u16*)d_in[0];
  char* ws = (char*)d_ws;
  k0_ln_mod<<<256, 256, 0, stream>>>(x, (const u16*)d_in[8], (const u16*)d_in[9], (const u16*)d_in[1], (const u16*)d_in[2],
                                     (const u16*)d_in[3], (const u16*)d_in[4], (const u16*)d_in[6], (const u16*)d_in[7], ws);
  k1_seq<<<256, NT, 0, stream>>>((const u16*)d_in[1], (const u16*)d_in[2], (const u16*)d_in[6], (const u16*)d_in[7], ws);
  k2_hid<<<256, NT, 0, stream>>>(x, (const u16*)d_in[3], (const u16*)d_in[4], ws);
  km_mlp<<<256, NT, 0, stream>>>((const u16*)d_in[6], (const u16*)d_in[7], ws);
  k5_out<<<256, 576, 0, stream>>>(ws, (u16*)d_out);
}
```

```cpp
#include <hip/hip_runtime.h>
#include <stdint.h>
#include <type_traits>

typedef unsigned short u16;
typedef __bf16 bf16x8 __attribute__((ext_vector_type(8)));
typedef __bf16 bf16x2 __attribute__((ext_vector_type(2)));
typedef float f32x4 __attribute__((ext_vector_type(4)));
typedef float f32x2 __attribute__((ext_vector_type(2)));
typedef unsigned u32x4 __attribute__((ext_vector_type(4)));
typedef unsigned u32x2 __attribute__((ext_vector_type(2)));
typedef short s16x4 __attribute__((ext_vector_type(4)));
#define LAS __attribute__((address_space(3)))
#define DEVI __device__ __forceinline__

constexpr int S = 2048, D = 576, F = 4608;
constexpr size_t WS_MODP = 0;
constexpr size_t WS_STAT = 192 << 10;
constexpr size_t WS_XNT  = 1 << 20;
constexpr size_t WS_PQ   = 4 << 20;
constexpr size_t WS_RSTD = 384 << 10;
constexpr size_t WS_UV   = 400 << 10;
constexpr size_t WS_X2P  = 14 << 20;
constexpr size_t WS_X2   = 17 << 20;
constexpr size_t WS_PART = 20 << 20;

#define WAIT_V(n) asm volatile("s_waitcnt vmcnt(%0)" ::"n"(n) : "memory")
#define WAIT_L0() do { __builtin_amdgcn_s_waitcnt(0xC07F); asm volatile("" ::: "memory"); } while (0)
#define PIN(x) asm volatile("" : "+v"(x))
#define BAR() do { asm volatile("" ::: "memory"); __builtin_amdgcn_s_barrier(); asm volatile("" ::: "memory"); } while (0)
#define SCHED() __builtin_amdgcn_sched_barrier(0)
#define SGB(m, n) __builtin_amdgcn_sched_group_barrier(m, n, 0)

#define STAMP(k, i)
#define LT(i)
#define LTDECL
#define LTOUT(k)
DEVI float bf_lo(unsigned v) { return __uint_as_float(v << 16); }
DEVI float bf_hi(unsigned v) { return __uint_as_float(v & 0xffff0000u); }
DEVI unsigned pk2(float a, float b) { bf16x2 p = {(__bf16)a, (__bf16)b}; return __builtin_bit_cast(unsigned, p); }
DEVI float fma1(float a, float b, float c) { float d; asm("v_fma_f32 %0, %1, %2, %3" : "=v"(d) : "v"(a), "v"(b), "v"(c)); return d; }
DEVI float rbf(float a) { return bf_lo(pk2(a, a) & 0xffffu); }
template <int CTRL> DEVI float dpp_add(float v) {
  int t = __builtin_amdgcn_update_dpp(0, __float_as_int(v), CTRL, 0xf, 0xf, true);
  return v + __int_as_float(t);
}
DEVI float x16_add(float v) {
  u32x2 r = __builtin_amdgcn_permlane16_swap(__float_as_uint(v), __float_as_uint(v), false, false);
  return __uint_as_float(r[0]) + __uint_as_float(r[1]);
}
DEVI float x32_add(float v) {
  u32x2 r = __builtin_amdgcn_permlane32_swap(__float_as_uint(v), __float_as_uint(v), false, false);
  return __uint_as_float(r[0]) + __uint_as_float(r[1]);
}
DEVI float row16_sum(float v) { v = dpp_add<0xB1>(v); v = dpp_add<0x4E>(v); v = dpp_add<0x141>(v); v = dpp_add<0x140>(v); return v; }
DEVI void glds16(const char* g, char* l) {
 __builtin_amdgcn_global_load_lds((const unsigned*)g, (LAS unsigned*)l, 16, 0, 0); }
constexpr int VB = 7168, GB = 3072;
#define GL(v, g, o) "global_load_lds_dwordx4 %" #v ", %" #g " offset:%" #o "\n\t"
template <int LO, int GO> DEVI void dma8(const char* g, const unsigned* v, unsigned l) {
  asm volatile("s_add_u32 m0, %0, %2\n\ts_nop 0\n\t" GL(3, 1, 11) GL(4, 1, 12) GL(5, 1, 13) GL(6, 1, 14) GL(7, 1, 15) GL(8, 1, 16) GL(9, 1, 17) GL(10, 1, 18)
               ::"s"(l), "s"(g), "n"(LO + 4096 - GO), "v"(v[0]), "v"(v[1]), "v"(v[2]), "v"(v[3]), "v"(v[4]), "v"(v[5]), "v"(v[6]), "v"(v[7]),
               "n"(GO - 4096), "n"(GO - 3072), "n"(GO - 2048), "n"(GO - 1024), "n"(GO), "n"(GO + 1024), "n"(GO + 2048), "n"(GO + 3072) : "memory", "scc");
}
template <int LO, int GO> DEVI void dma5(const char* g, const unsigned* v, unsigned l) {
  asm volatile("s_add_u32 m0, %0, %2\n\ts_nop 0\n\t" GL(3, 1, 8) GL(4, 1, 9) GL(5, 1, 10) GL(6, 1, 11) GL(7, 1, 12)
               ::"s"(l), "s"(g), "n"(LO + 4096 - GO), "v"(v[0]), "v"(v[1]), "v"(v[2]), "v"(v[3]), "v"(v[4]),
               "n"(GO - 4096), "n"(GO - 3072), "n"(GO - 2048), "n"(GO - 1024), "n"(GO) : "memory", "scc");
}
template <int LO, int GO> DEVI void dma4(const char* g, const unsigned* v, unsigned l) {
  asm volatile("s_add_u32 m0, %0, %2\n\ts_nop 0\n\t" GL(3, 1, 7) GL(4, 1, 8) GL(5, 1, 9) GL(6, 1, 10)
               ::"s"(l), "s"(g), "n"(LO + 4096 - GO), "v"(v[0]), "v"(v[1]), "v"(v[2]), "v"(v[3]),
               "n"(GO - 4096), "n"(GO - 3072), "n"(GO - 2048), "n"(GO - 1024) : "memory", "scc");
}
template <int LO, int GO> DEVI void dma2(const char* g, const unsigned* v, unsigned l) {
  asm volatile("s_add_u32 m0, %0, %2\n\ts_nop 0\n\t" GL(3, 1, 5) GL(4, 1, 6)
               ::"s"(l), "s"(g), "n"(LO + 4096 - GO), "v"(v[0]), "v"(v[1]), "n"(GO - 4096), "n"(GO - 3072) : "memory", "scc");
}
template <int LO, int GO> DEVI void dma1(const char* g, unsigned v, unsigned l) {
  asm volatile("s_add_u32 m0, %0, %2\n\ts_nop 0\n\t" GL(3, 1, 4) ::"s"(l), "s"(g), "n"(LO + 4096 - GO), "v"(v), "n"(GO - 4096) : "memory", "scc");
}
#define PINS(x) asm volatile("" : "+s"(x))
template <int N, class Fn> DEVI void static_for(Fn&& f) {
  if constexpr (N > 0) { static_for<N - 1>(f); f(std::integral_constant<int, N - 1>{}); }
}
DEVI unsigned touch(const char* p) { unsigned v; asm volatile("global_load_dword %0, %1, off" : "=v"(v) : "v"(p)); return v; }
DEVI bf16x8 lds_frag(const char* p) { return *(const LAS bf16x8*)p; }
DEVI bf16x8 lds_frag_tr(const char* p, int off2) {
  s16x4 a = __builtin_amdgcn_ds_read_tr16_b64_v4i16((LAS s16x4*)p);
  s16x4 b = __builtin_amdgcn_ds_read_tr16_b64_v4i16((LAS s16x4*)(p + off2));
  return __builtin_bit_cast(bf16x8, __builtin_shufflevector(a, b, 0, 1, 2, 3, 4, 5, 6, 7));
}
DEVI f32x4 mfma(bf16x8 a, bf16x8 b, f32x4 c) {
 return __builtin_amdgcn_mfma_f32_16x16x32_bf16(a, b, c, 0, 0, 0); }
DEVI float mod_at(const float* modp, int i) { return rbf((modp[i] + modp[3456 + i]) + (modp[2 * 3456 + i] + modp[3 * 3456 + i])); }

#define PF_ST 8
__global__ __launch_bounds__(256) void k0_ln_mod(const u16* __restrict__ x, const u16* __restrict__ cvec,
                                                 const u16* __restrict__ wmod, const u16* __restrict__ wsre, const u16* __restrict__ wsim,
                                                 const u16* __restrict__ wdre, const u16* __restrict__ wdim, char* __restrict__ ws) {
  __shared__ __attribute__((aligned(16))) char lds[8 * 1216 + 4 * 64 * 4];
  const int tid = threadIdx.x, lane = tid & 63, w = __builtin_amdgcn_readfirstlane(tid >> 6), b = blockIdx.x;
  STAMP(0, 0);
  const int tok = 8 * b + 2 * w + (lane >> 5), i = lane & 31;
  const unsigned* x32 = (const unsigned*)x + tok * 288 + i;
  unsigned xr[9];
#pragma unroll
  for (int n = 0; n < 9; n++) xr[n] = x32[32 * n];
  const bool domod = b < 216;
  const int cg = b % 54, kq = b / 54;
  u32x4 wv[5]; u16 cv[5];
  if (domod) {
    const int r0 = tid >> 3, ch = tid & 7;
#pragma unroll
    for (int it = 0; it < 5; it++) {
      int r = r0 + 32 * it; if (r > 143) r = 143;
      r += kq * 144;
      wv[it] = *(const u32x4*)(wmod + (size_t)r * 3456 + cg * 64 + ch * 8);
      cv[it] = cvec[r];
    }
  }
  unsigned tv[2];
  {
    const int q0 = tid & 127;
    const size_t o = (size_t)(8 * b + (q0 >> 4)) * 4096 + ((q0 >> 3) & 1) * 2048 + (q0 & (PF_ST - 1)) * 128;
    tv[0] = touch((const char*)(tid < 128 ? wsre : wsim) + o);
    int q = (b >> 1) * 41 + (tid < 41 ? tid : 40); if (q > 5183) q = 5183;
    if (w == 0) tv[1] = touch((const char*)((b & 1) ? wdim : wdre) + q * 128); else tv[1] = 0;
  }
  float v[18], s = 0.f;
#pragma unroll
  for (int n = 0; n < 9; n++) { v[2 * n] = bf_lo(xr[n]); v[2 * n + 1] = bf_hi(xr[n]); s += v[2 * n] + v[2 * n + 1]; }
  STAMP(0, 1);
  s = x16_add(row16_sum(s));
  const float mean = s * (1.f / D);
  float q = 0.f;
#pragma unroll
  for (int n = 0; n < 18; n++) { v[n] -= mean; q += v[n] * v[n]; }
  q = x16_add(row16_sum(q));
  const float rs = rsqrtf(q * (1.f / D) + 1e-5f);
  {
    char* row = lds + (2 * w + (lane >> 5)) * 1216 + i * 4;
#pragma unroll
    for (int n = 0; n < 9; n++) *(LAS unsigned*)(row + 128 * n) = pk2(v[2 * n] * rs, v[2 * n + 1] * rs);
  }
  float* red = (float*)(lds + 8 * 1216);
  if (domod) {
    float a[8] = {0, 0, 0, 0, 0, 0, 0, 0};
#pragma unroll
    for (int it = 0; it < 5; it++) {
      float c = bf_lo(cv[it]);
      float sl = rbf(c / (1.f + __expf(-c)));
      if (it == 4 && (tid >> 3) >= 16) sl = 0.f;
#pragma unroll
      for (int e = 0; e < 4; e++) { a[2 * e] += sl * bf_lo(wv[it][e]); a[2 * e + 1] += sl * bf_hi(wv[it][e]); }
    }
#pragma unroll
    for (int e = 0; e < 8; e++) a[e] = x32_add(x16_add(dpp_add<0x128>(a[e])));
    if (lane < 8) {
      *(LAS f32x4*)(red + w * 64 + lane * 8) = f32x4{a[0], a[1], a[2], a[3]};
      *(LAS f32x4*)(red + w * 64 + lane * 8 + 4) = f32x4{a[4], a[5], a[6], a[7]};
    }
  }
  __syncthreads();
  STAMP(0, 2);
  if (domod && tid < 64) {
    float* modp = (float*)(ws + WS_MODP);
    modp[kq * 3456 + cg * 64 + tid] = (red[tid] + red[64 + tid]) + (red[128 + tid] + red[192 + tid]);
  }
  const int grp = lane >> 4, c = lane & 15;
  for (int it = w; it < 9; it += 4) {
    const char* p = lds + (c >> 2) * 1216 + (64 * it + 16 * grp + 4 * (c & 3)) * 2;
    bf16x8 f = lds_frag_tr(p, 4 * 1216);
    const int J = 4 * it + grp, nb = J / 9, j = J - nb * 9;
    char* dst = ws + WS_XNT + ((size_t)((nb * 64 + (b >> 2)) * 9 + j) << 10) + (((b & 3) * 16 + c) << 4);
    *(bf16x8*)dst = f;
  }
  WAIT_V(0);
  asm volatile("" ::"v"(tv[0]), "v"(tv[1]));
  STAMP(0, 3);
}

constexpr int NL = 2;
constexpr int NT = 256 + 64 * NL;
#define K1_ROT 1
#define K1_MAP 0
constexpr int K1_SLOT = 34816, K1_NS = 4, K1_NST = 16;
#define XV 0
#define K1_X0 4
__global__ __launch_bounds__(NT) void k1_seq(const u16* __restrict__ wsre, const u16* __restrict__ wsim, const u16* __restrict__ w1,
                                             char* __restrict__ ws) {
  __shared__ __attribute__((aligned(16))) char lds[K1_NS * K1_SLOT + 1024 + 144 * 8 + 512 + 16 + 18432 + 2304 + 256];
  char* const ones = lds + K1_NS * K1_SLOT;
  float* const sca = (float*)(ones + 1024);
  float* const ssa = sca + 144;
  float* const srs = ssa + 144;
  volatile LAS int* const sflag = (volatile LAS int*)(srs + 128);
  const int tid = threadIdx.x, lane = tid & 63, w = __builtin_amdgcn_readfirstlane(tid >> 6);
  const int xcd = blockIdx.x & 7, idx = blockIdx.x >> 3;
  int ks, nb, mb;
  STAMP(1, 0);
  if (K1_MAP == 0) { ks = idx & 1; nb = (idx >> 1) & 3; const int ms = idx >> 3; mb = (ms >> 1) * 16 + 2 * xcd + (ms & 1); }
  else { ks = xcd & 1; nb = idx & 3; const int ms = idx >> 2; mb = (ms >> 2) * 16 + 4 * (xcd >> 1) + (ms & 3); }
  if (w >= 4) {
    const int li = w - 4;
    const char* wsb = (const char*)(mb >= 16 ? wsim : wsre) + (size_t)(mb & 15) * 128 * 4096 + ks * 2048 - GB;
    const char* xb = ws + WS_XNT + (size_t)(nb * 64 + ks * 32) * 9216 - GB;
    const unsigned vw = (lane >> 3) * 4096 + (((lane & 7) ^ (lane >> 3)) << 4), vc = (lane << 4) + VB;
    unsigned vo[8];
#pragma unroll
    for (int p = 0; p < 8; p++) vo[p] = li ? vc : vw + p * (32768 - 1024) + VB;
    const unsigned l0 = (unsigned)(size_t)(LAS char*)lds + li * 17408;
    const char *g0, *g1, *g2;
    auto nxt = [&](int i) {
      const int s = i < 2 ? (2 * nb + i) & 7 : i < 4 ? 6 + 2 * nb + i : i < 10 ? (2 * nb + i - 2) & 7 : 8 + ((2 * nb + i - 8) & 7);
      const char* gx = xb + s * 18432;
      g0 = li ? gx + 1024 : wsb + s * 128;
      g1 = g0 + (li ? 8192 : 8 * 32768);
      g2 = gx + li * 17408;
      PINS(g0); PINS(g1); PINS(g2);
    };
    LTDECL; LT(0);
    nxt(0);
#pragma nounroll
    for (int i = 0; i < K1_NST; i++) {
      const unsigned slot = l0 + (i & 3) * K1_SLOT;
      dma8<0, 0>(g0, vo, slot); dma8<8192, 0>(g1, vo, slot); dma1<16384, 0>(g2, vc, slot);
      nxt(i + 1);
      if (i >= 2) {
        WAIT_V(34);
        BAR();
      }
    }
    if (idx >= 18) { WAIT_V(17); BAR(); WAIT_V(0); BAR(); LTOUT(6); return; }
    char* const jw = li ? lds : (char*)(srs + 128) + 16;
    char* const jt = (char*)(srs + 128) + 16 + 18432 + li * 1152;
    char* const jx = (char*)(srs + 128) + 16 + 18432 + 2304;
    const int g = lane >> 4, c = lane & 15;
    f32x4 q[12];
    unsigned eo[3];
#pragma unroll
    for (int r = 0; r < 3; r++) {
      const unsigned e = r * 64 + lane < 144 ? r * 64 + lane : 143;
      eo[r] = e;
#pragma unroll
      for (int i = 0; i < 4; i++)
        asm volatile("global_load_dwordx4 %0, %1, %2" : "=v"(q[4 * r + i]) : "v"(e < 72 ? 2304 + e * 16 : e * 16 - 1152), "s"(ws + WS_MODP + (3 * 576 + li * 288 + i * 3456) * 4) : "memory");
    }
    const char* xs = (const char*)w1 + (size_t)li * 288 * 9216 + xcd * 1152 + idx * 64 - GB;
#pragma unroll
    for (int p = 0; p < 8; p++) vo[p] = ((lane >> 2) + 16 * p) * 9216 + (lane & 3) * 16 + VB - 1024 * p;
    const unsigned lx = (unsigned)(size_t)(LAS char*)jw;
    dma8<0, 0>(xs, vo, lx);
    WAIT_V(37); BAR();
    dma8<8192, 0>(xs + 128 * 9216, vo, lx); dma2<16384, 0>(xs + 256 * 9216, vo, lx);
    WAIT_V(30); BAR();
    WAIT_V(18);
#pragma unroll
    for (int e = 0; e < 12; e++) PIN(q[e]);
#pragma unroll
    for (int r = 0; r < 3; r++) {
      f32x4 t = (q[4 * r] + q[4 * r + 1]) + (q[4 * r + 2] + q[4 * r + 3]);
#pragma unroll
      for (int e = 0; e < 4; e++) t[e] = eo[r] < 72 ? rbf(t[e]) + 1.f : t[e];
      *(LAS u32x2*)(jt + eo[r] * 8) = u32x2{pk2(t[0], t[1]), pk2(t[2], t[3])};
    }
    WAIT_V(0);
    {
      const char* pa = jw + (4 * g + (c >> 2)) * 64 + (c & 3) * 8;
      const char* pt = jt + (c & 1) * 576 + g * 8;
      f32x4 d0 = {0, 0, 0, 0}, d1 = {0, 0, 0, 0};
#pragma unroll
      for (int k = 0; k < 9; k++) {
        const u32x2 lo = *(const LAS u32x2*)(pt + k * 64), hi = *(const LAS u32x2*)(pt + k * 64 + 32);
        const bf16x8 bv = __builtin_bit_cast(bf16x8, u32x4{lo[0], lo[1], hi[0], hi[1]});
        d0 = mfma(lds_frag_tr(pa + k * 2048, 1024), bv, d0); d1 = mfma(lds_frag_tr(pa + k * 2048 + 32, 1024), bv, d1);
      }
      char* const px = jx + (g * 2 + (c & 1)) * 16;
      if (c < 2) *(LAS f32x4*)(px + li * 128) = li ? d0 : d1;
      WAIT_L0();
      if (lane == 0) sflag[2 + li] = 1;
      for (int it = 0; it < (1 << 20) && sflag[3 - li] == 0; it++) __builtin_amdgcn_s_sleep(1);
      asm volatile("" ::: "memory");
      if (c < 2) *(f32x4*)(ws + WS_UV + (c * F + xcd * 576 + idx * 32 + li * 16 + g * 4) * 4) = (li ? d1 : d0) + *(const LAS f32x4*)(px + (li ^ 1) * 128);
    }
    LTOUT(6);
    return;
  }
  const int mh = w >> 1, nh = w & 1, g = lane >> 4, c = lane & 15;
  float mq[8];
  {
    const float* modp = (const float*)(ws + WS_MODP) + nb * 144 + (tid < 144 ? tid : 0);
#pragma unroll
    for (int i = 0; i < 8; i++) mq[i] = modp[(i >> 1) * 3456 + (i & 1) * 576];
    *(LAS unsigned*)(ones + tid * 4) = 0x3f803f80u;
    if (tid < 4) sflag[tid] = 0;
  }
  f32x4 acc[4][5];
#pragma unroll
  for (int m = 0; m < 4; m++)
#pragma unroll
    for (int j = 0; j < 5; j++) { acc[m][j] = f32x4{0, 0, 0, 0}; PIN(acc[m][j]); }
  const int vb = (64 * mh + c) * 128 + ((g ^ (c & 7)) << 4);
  const int va = 16384 + nh * 5120 + (lane << 4);
  bf16x8 B0[4], A0[5], B1[4], A1[5];
  auto rd = [&](bf16x8* B, bf16x8* A, int s, int hk) {
    const char* slot = lds + (s % K1_NS) * K1_SLOT;
    const char* pb = slot + (vb ^ (hk << 6));
    const char* pa = slot + va + hk * 9216;
    const char* p4 = nh ? ones + (lane << 4) : pa + 4096;
    B[0] = lds_frag(pb); A[0] = lds_frag(pa);
#pragma unroll
    for (int m = 1; m < 4; m++) B[m] = lds_frag(pb + m * 2048);
#pragma unroll
    for (int j = 1; j < 4; j++) A[j] = lds_frag(pa + j * 1024);
    A[4] = lds_frag(p4);
  };
  auto mm = [&](bf16x8* B, bf16x8* A) {
#pragma unroll
    for (int j = 0; j < 5; j++)
#pragma unroll
      for (int m = 0; m < 4; m++) acc[m][j] = mfma(A[j], B[m], acc[m][j]);
  };
  auto pat = [&]() {
#pragma unroll
    for (int i = 0; i < 9; i++) { SGB(0x008, 1); SGB(0x100, 1); }
    SGB(0x008, 11);
  };
  WAIT_L0();
  BAR();
  STAMP(1, 1);
  rd(B0, A0, 0, 0);
  if (tid < 144) { ssa[tid] = rbf((mq[0] + mq[2]) + (mq[4] + mq[6])); sca[tid] = rbf(1.f + rbf((mq[1] + mq[3]) + (mq[5] + mq[7]))); }
  for (int s = 0; s < K1_NST; s++) {
    SCHED();
    rd(B1, A1, s, 1); mm(B0, A0); pat();
    SCHED();
    if (s + 1 < K1_NST) {
      WAIT_L0(); BAR();
      SCHED();
      rd(B0, A0, s + 1, 0); mm(B1, A1); pat();
    } else {
      mm(B1, A1);
    }
  }
  SCHED();
  STAMP(1, 2);
  if (nh == 1) {
    if (g == 0) {
#pragma unroll
      for (int m = 0; m < 4; m++) srs[64 * mh + 16 * m + c] = acc[m][4][0];
    }
    WAIT_L0();
    if (lane == 0) sflag[mh] = 1;
  } else {
    for (int it = 0; it < (1 << 20) && sflag[mh] == 0; it++) __builtin_amdgcn_s_sleep(1);
  }
  asm volatile("" ::: "memory");
  float rsum[4];
#pragma unroll
  for (int m = 0; m < 4; m++) rsum[m] = srs[64 * mh + 16 * m + c];
  const float sgn = mb >= 16 ? -1.f : 1.f;
  const int tb = (mb & 15) * 4 + 2 * mh;
  char* const pq = ws + WS_PQ;
#pragma unroll
  for (int j = 0; j < 5; j++) {
    if (j == 4 && nh) break;
    const int jj = 5 * nh + j;
    f32x4 ca = *(const LAS f32x4*)(sca + 16 * jj + 4 * g), sa = *(const LAS f32x4*)(ssa + 16 * jj + 4 * g);
    ca *= sgn; sa *= sgn;
    const int Jp = (mb >> 4) * 36 + 9 * nb + jj;
#pragma unroll
    for (int mp = 0; mp < 2; mp++) {
      f32x4 p0 = acc[2 * mp][j] * ca + rsum[2 * mp] * sa, p1 = acc[2 * mp + 1][j] * ca + rsum[2 * mp + 1] * sa;
      u32x2 lo = __builtin_amdgcn_permlane16_swap(pk2(p0[0], p0[1]), pk2(p1[0], p1[1]), false, false);
      u32x2 hi = __builtin_amdgcn_permlane16_swap(pk2(p0[2], p0[3]), pk2(p1[2], p1[3]), false, false);
      u32x4 o = {lo[0], hi[0], lo[1], hi[1]};
      const unsigned off = (unsigned)(((((tb + mp) * 36 + (Jp >> 1)) * 2 + ks) * 2 + (g & 1)) << 10) + ((((Jp & 1) * 2 + (g >> 1)) * 16 + c) << 4);
      *(u32x4*)(pq + off) = o;
    }
  }
  STAMP(1, 3);
}

#define K2_ROT 0
#define K2_X0 3
constexpr int K2_SLOT = 26624, K2_NS = 4, K2_NST = 18;
__global__ __launch_bounds__(NT) void k2_hid(const u16* __restrict__ x, const u16* __restrict__ wdre, const u16* __restrict__ wdim,
                                             char* __restrict__ ws) {
  __shared__ __attribute__((aligned(16))) char lds[K2_NS * K2_SLOT + 144 * 2 * 4];
  float* const sga = (float*)(lds + K2_NS * K2_SLOT);
  float* const scb = sga + 144;
  const int tid = threadIdx.x, lane = tid & 63, w = __builtin_amdgcn_readfirstlane(tid >> 6);
  const int xcd = blockIdx.x & 7, idx = blockIdx.x >> 3;
  const int nb = idx & 3, tb = xcd * 8 + (idx >> 2);
  STAMP(2, 0);
  if (w >= 4) {
    const int li = w - 4;
    const unsigned vw = (lane >> 3) * 1152 + (((lane & 7) ^ (lane >> 3)) << 4), vc = (lane << 4) + VB;
    const char* pb = ws + WS_PQ + (size_t)tb * 36 * 4096 - GB;
    unsigned va[8], vb[5];
#pragma unroll
    for (int p = 0; p < 8; p++) va[p] = li ? vc : vw + p * (9216 - 1024) + VB;
#pragma unroll
    for (int p = 0; p < 5; p++) vb[p] = vw + (8 + 5 * li) * 9216 + p * (9216 - 1024) + VB;
    const unsigned l0 = (unsigned)(size_t)(LAS char*)lds;
    const int rot = K2_ROT ? (2 * (idx >> 2) + 5 * nb) % 18 : 0;
    const char *g0, *gw;
    auto nxt = [&](int i) {
      int s = i + rot; if (s >= 18) s -= 18;
      gw = (const char*)(s >= 9 ? wdim : wdre) + nb * 144 * 1152 + (s >= 9 ? s - 9 : s) * 128 - GB;
      g0 = li ? pb + s * 8192 : gw;
      PINS(g0); PINS(gw);
    };
    nxt(0);
#pragma nounroll
    for (int i = 0; i < K2_NST; i++) {
      const unsigned slot = l0 + (i & 3) * K2_SLOT;
      dma8<0, 0>(g0, va, slot + li * 18432);
      dma5<8192, 0>(gw, vb, slot + li * 5120);
      nxt(i + 1);
      if (i >= 2) { WAIT_V(26); BAR(); }
    }
    WAIT_V(13); BAR(); WAIT_V(0); BAR();
    BAR(); BAR();
    return;
  }
  const int mh = w >> 1, kh = w & 1, g = lane >> 4, c = lane & 15;
  u32x2 xr[5];
  const int tok = tb * 32 + mh * 16 + c;
#pragma unroll
  for (int jj = 0; jj < 5; jj++) {
    int j = kh * 5 + jj; if (j > 8) j = 8;
    xr[jj] = *(const u32x2*)(x + (size_t)tok * D + nb * 144 + 16 * j + 4 * g);
  }
  float mq[8];
  {
    const float* modp = (const float*)(ws + WS_MODP) + 2 * 576 + nb * 144 + (tid < 144 ? tid : 0);
#pragma unroll
    for (int i = 0; i < 8; i++) mq[i] = modp[(i >> 1) * 3456 + (i & 1) * 1152];
  }
  f32x4 acc[9];
#pragma unroll
  for (int j = 0; j < 9; j++) { acc[j] = f32x4{0, 0, 0, 0}; PIN(acc[j]); }
  const int va = c * 128 + (((4 * kh + g) ^ (c & 7)) << 4);
  const int vb = 18432 + kh * 4096 + mh * 1024 + (lane << 4);
  bf16x8 Bx[2], By[2], A0[5], A1[4];
  auto rd0 = [&](bf16x8* B, int s) {
    const char* slot = lds + (s % K2_NS) * K2_SLOT;
    B[0] = lds_frag(slot + vb); A0[0] = lds_frag(slot + va); B[1] = lds_frag(slot + vb + 2048);
#pragma unroll
    for (int j = 1; j < 5; j++) A0[j] = lds_frag(slot + va + j * 2048);
  };
  auto rd1 = [&](int s) {
    const char* slot = lds + (s % K2_NS) * K2_SLOT;
#pragma unroll
    for (int j = 0; j < 4; j++) A1[j] = lds_frag(slot + va + (5 + j) * 2048);
  };
  auto mm0 = [&](bf16x8* B) {
#pragma unroll
    for (int j = 0; j < 5; j++) { acc[j] = mfma(A0[j], B[0], acc[j]); }
#pragma unroll
    for (int j = 0; j < 5; j++) { acc[j] = mfma(A0[j], B[1], acc[j]); }
  };
  auto mm1 = [&](bf16x8* B) {
#pragma unroll
    for (int j = 0; j < 4; j++) { acc[5 + j] = mfma(A1[j], B[0], acc[5 + j]); }
#pragma unroll
    for (int j = 0; j < 4; j++) { acc[5 + j] = mfma(A1[j], B[1], acc[5 + j]); }
  };
  auto step = [&](bf16x8* Bc, bf16x8* Bn, int s, bool last) {
    SCHED();
    rd1(s); mm0(Bc);
#pragma unroll
    for (int i = 0; i < 4; i++) { SGB(0x008, 1); SGB(0x100, 1); }
    SGB(0x008, 6);
    SCHED();
    if (!last) {
      WAIT_L0(); BAR();
      SCHED();
      rd0(Bn, s + 1); mm1(Bc);
#pragma unroll
      for (int i = 0; i < 7; i++) { SGB(0x008, 1); SGB(0x100, 1); }
      SGB(0x008, 1);
    } else mm1(Bc);
  };
  BAR();
  STAMP(2, 1);
  rd0(Bx, 0);
  for (int s = 0; s < K2_NST - 2; s += 2) { step(Bx, By, s, false); step(By, Bx, s + 1, false); }
  step(Bx, By, K2_NST - 2, false); step(By, Bx, K2_NST - 1, true);
  SCHED();
  STAMP(2, 2);
  if (tid < 144) { sga[tid] = rbf((mq[0] + mq[2]) + (mq[4] + mq[6])); scb[tid] = rbf(1.f + rbf((mq[1] + mq[3]) + (mq[5] + mq[7]))); }
  WAIT_L0(); BAR();
  {
    char* xb = lds + mh * 9216 + (lane << 4);
    if (kh == 0) {
#pragma unroll
      for (int j = 5; j < 9; j++) *(LAS f32x4*)(xb + j * 1024) = acc[j];
    } else {
#pragma unroll
      for (int j = 0; j < 5; j++) *(LAS f32x4*)(xb + j * 1024) = acc[j];
    }
    WAIT_L0(); BAR();
    if (kh == 0) {
#pragma unroll
      for (int j = 0; j < 5; j++) acc[j] += *(const LAS f32x4*)(xb + j * 1024);
    } else {
#pragma unroll
      for (int j = 0; j < 4; j++) acc[j] = acc[5 + j] + *(const LAS f32x4*)(xb + (5 + j) * 1024);
    }
  }
  const int nj = 5 - kh;
  float sum = 0.f;
#pragma unroll
  for (int jj = 0; jj < 5; jj++) {
    if (jj < nj) {
      const int j = kh * 5 + jj;
      f32x4 ga = *(const LAS f32x4*)(sga + 16 * j + 4 * g), cb = *(const LAS f32x4*)(scb + 16 * j + 4 * g);
      f32x4 xv = {bf_lo(xr[jj][0]), bf_hi(xr[jj][0]), bf_lo(xr[jj][1]), bf_hi(xr[jj][1])};
      f32x4 x2 = xv + ga * acc[jj];
      acc[jj] = x2;
      sum += (x2[0] + x2[1]) + (x2[2] + x2[3]);
      u32x2 o = {pk2(x2[0], x2[1]), pk2(x2[2], x2[3])};
      *(u32x2*)(ws + WS_X2 + (((((tb * 4 + nb) * 9 + j) * 2 + mh) * 64 + lane) << 3)) = o;
      f32x4 xs = x2 * cb;
      u32x2 o2 = {pk2(xs[0], xs[1]), pk2(xs[2], xs[3])};
      const int J = 9 * nb + j;
      *(u32x2*)(ws + WS_X2P + ((((tb >> 1) * 18 + (J >> 1)) * 4 + (tb & 1) * 2 + mh) << 10) + (lane << 4) + (J & 1) * 8) = o2;
    }
  }
  sum = x32_add(x16_add(sum));
  const float lm = sum * (kh ? 1.f / 64 : 1.f / 80);
  float m2 = 0.f;
#pragma unroll
  for (int jj = 0; jj < 5; jj++) {
    if (jj < nj) {
      f32x4 d = acc[jj] - lm;
      m2 += (d[0] * d[0] + d[1] * d[1]) + (d[2] * d[2] + d[3] * d[3]);
    }
  }
  m2 = x32_add(x16_add(m2));
  if (g == 0) *(f32x2*)(ws + WS_STAT + (((nb * 2 + kh) * S + tok) << 3)) = f32x2{sum, m2};
  STAMP(2, 3);
}

#define KM_ROT 1
#define NO_TCH
constexpr int KM_ACT = 73728, KM_SLOT = 20480;
__global__ __launch_bounds__(NT) void km_mlp(const u16* __restrict__ w1, const u16* __restrict__ w2, char* __restrict__ ws) {
  __shared__ __attribute__((aligned(16))) char lds[KM_ACT + 4 * KM_SLOT + 256];
  char* const ring = lds + KM_ACT;
  const int tid = threadIdx.x, lane = tid & 63, w = __builtin_amdgcn_readfirstlane(tid >> 6);
  const int fs = blockIdx.x & 7, tt = blockIdx.x >> 3;
  const int rot = KM_ROT ? (tt * 18) >> 5 : 0;
  STAMP(3, 0);
  if (w >= 4) {
    const int li = w - 4;
    const unsigned sw = ((lane & 7) ^ (((lane >> 4) & 3) << 1)) << 4, vc = (lane << 4) + VB;
    unsigned vo[8];
    const unsigned vx[4] = {vc, vc, vc, vc};
#pragma unroll
    for (int p = 0; p < 8; p++) vo[p] = ((lane >> 3) + (p & 1) * 8) * 9216 + sw + (p >> 1) * 128 + VB - 1024 * p;
    const char* xb = ws + WS_X2P + (size_t)tt * KM_ACT - GB;
    const char* w1b = (const char*)w1 + fs * 1152 + li * 16 * 9216 - GB;
    const char* w2b = (const char*)w2 + (size_t)fs * 576 * 1152 - GB;
    const unsigned l0 = (unsigned)(size_t)(LAS char*)lds, lr = l0 + KM_ACT + li * 10240;
    int k = rot;
    const char* src = w1b + k * (32 * 9216);
    auto h0 = [&](auto par) { dma8<par * 2 * KM_SLOT, 0>(src, vo, lr); dma2<par * 2 * KM_SLOT + 8192, 512>(src, vo, lr); };
    auto h1 = [&](auto par) { dma8<(par * 2 + 1) * KM_SLOT, 640>(src, vo, lr); };
    auto nx1 = [&]() { k++; src += 32 * 9216; if (k == 18) { k = 0; src = w1b; } PINS(src); };
    auto nx2 = [&]() {
      src = w2b + (k < 16 ? 128 * (k >> 2) + 64 * li + 16 * (k & 3) : 32 * k + 16 * li) * 1152; PINS(src);
      k = k == 17 ? 0 : k + 1;
    };
    auto burst = [&](int pos) {
#pragma unroll
      for (int e = 0; e < 2; e++) {
        const int q = pos + 2 * li + e; int kx = q + rot; if (kx >= 18) kx -= 18;
        dma4<0, 0>(xb + kx * 4096, vx, l0 + q * 4096);
      }
    };
    const std::integral_constant<int, 0> P0; const std::integral_constant<int, 1> P1;
    LTDECL; LT(0);
    dma2<0, 0>(xb + k * 4096 + li * 2048, vx, l0 + li * 2048); h0(P0); LT(1); h1(P0); nx1();
    dma2<4096, 0>(xb + k * 4096 + li * 2048, vx, l0 + li * 2048); h0(P1);
    LT(2);
    WAIT_V(20);
    LT(3);
    BAR(); h1(P1); burst(2); nx1();
#pragma nounroll
    for (int s = 2; s < 18; s += 4) {
      WAIT_V(26); BAR(); h0(P0); WAIT_V(26); BAR(); h1(P0); nx1();
      WAIT_V(26); BAR(); h0(P1); WAIT_V(18); BAR(); h1(P1); nx1();
      WAIT_V(18); BAR(); h0(P0); WAIT_V(18); BAR(); h1(P0); nx1();
      WAIT_V(18); BAR(); h0(P1); WAIT_V(18); BAR(); h1(P1); if (s < 14) burst(s + 4); nx1();
    }
#pragma unroll
    for (int p = 0; p < 8; p++) { vo[p] = ((lane >> 3) + (p & 1) * 8) * 1152 + sw + (p >> 1) * 128 + VB - 1024 * p; PIN(vo[p]); }
    nx2();
#pragma nounroll
    for (int s = 0; s < 18; s += 2) {
      WAIT_V(18); BAR(); h0(P0); WAIT_V(18); BAR(); h1(P0); nx2();
      WAIT_V(18); BAR(); h0(P1); WAIT_V(18); BAR(); h1(P1); nx2();
    }
    WAIT_V(18); BAR(); WAIT_V(8); BAR(); WAIT_V(0); BAR();
    LTOUT(7);
    return;
  }
  const int g = lane >> 4, c = lane & 15;
  f32x4 acc[4][9];
  {
    f32x2 p[8]; f32x4 u[9], v[9];
    const f32x2* st = (const f32x2*)(ws + WS_STAT) + tt * 64 + lane;
#pragma unroll
    for (int i = 0; i < 8; i++) p[i] = st[i * S];
    const f32x4* uv = (const f32x4*)(ws + WS_UV) + fs * 144 + 4 * w + g;
#pragma unroll
    for (int j = 0; j < 9; j++) { u[j] = uv[16 * j]; v[j] = uv[F / 4 + 16 * j]; }
    SCHED();
    float sum = 0.f;
#pragma unroll
    for (int i = 0; i < 8; i++) sum += p[i][0];
    const float mean = sum * (1.f / D);
    float m2 = 0.f;
#pragma unroll
    for (int i = 0; i < 8; i++) {
      const float n = (i & 1) ? 64.f : 80.f, d = p[i][0] * ((i & 1) ? 1.f / 64 : 1.f / 80) - mean;
      m2 += p[i][1] + n * d * d;
    }
    const float var = m2 * (1.f / D) + 1e-5f, rs = rsqrtf(var);
    if (fs == 0 && w == 0) ((float*)(ws + WS_RSTD))[tt * 64 + lane] = rs;
    float nm[4], sd[4];
#pragma unroll
    for (int m = 0; m < 4; m++) {
      nm[m] = __int_as_float(__builtin_amdgcn_ds_bpermute((16 * m + c) << 2, __float_as_int(-mean)));
      sd[m] = __int_as_float(__builtin_amdgcn_ds_bpermute((16 * m + c) << 2, __float_as_int(var * rs)));
    }
#pragma unroll
    for (int j = 0; j < 9; j++) {
#pragma unroll
      for (int m = 0; m < 4; m++) { acc[m][j] = sd[m] * v[j] + nm[m] * u[j]; PIN(acc[m][j]); }
    }
  }
  const int va = KM_ACT + (4 * g + (c >> 2)) * 128 + ((w ^ ((2 * g + (c >> 3)) & 3)) << 5) + (c & 3) * 8;
  const int vb = lane << 4;
  bf16x8 Bx[4], By[4], A0[5], A1[4];
  auto rdA0 = [&](int par) {
#pragma unroll
    for (int j = 0; j < 5; j++) A0[j] = lds_frag_tr(lds + va + par * 2 * KM_SLOT + j * 2048, 10240);
  };
  auto rdA1 = [&](int par) {
#pragma unroll
    for (int j = 0; j < 4; j++) A1[j] = lds_frag_tr(lds + va + (par * 2 + 1) * KM_SLOT + j * 2048, 10240);
  };
  auto rdB = [&](bf16x8* B, int kb) {
#pragma unroll
    for (int m = 0; m < 4; m++) B[m] = lds_frag(lds + vb + kb * 4096 + m * 1024);
  };
  auto step = [&](bf16x8* Bc, bf16x8* Bn, int kb, int par, int mode) {
    const f32x4 zero = {0, 0, 0, 0};
    WAIT_L0(); BAR();
    SCHED();
    if (mode == 1) { rdB(Bc, kb); SCHED(); }
    rdA1(par);
#pragma unroll
    for (int j = 0; j < 5; j++)
#pragma unroll
      for (int m = 0; m < 4; m++) acc[m][j] = mfma(A0[j], Bc[m], mode == 1 ? zero : acc[m][j]);
#pragma unroll
    for (int i = 0; i < 8; i++) { SGB(0x008, 1); SGB(0x100, 1); }
    SGB(0x008, 12);
    SCHED();
    if (mode != 3) {
      WAIT_L0(); BAR();
      SCHED();
      rdA0(par ^ 1);
      if (mode != 2) rdB(Bn, kb + 1);
    }
#pragma unroll
    for (int j = 0; j < 4; j++)
#pragma unroll
      for (int m = 0; m < 4; m++) acc[m][5 + j] = mfma(A1[j], Bc[m], mode == 1 ? zero : acc[m][5 + j]);
    if (mode < 2) {
#pragma unroll
      for (int i = 0; i < 7; i++) { SGB(0x008, 1); SGB(0x100, 2); }
      SGB(0x008, 9);
    } else if (mode == 2) {
#pragma unroll
      for (int i = 0; i < 5; i++) { SGB(0x008, 1); SGB(0x100, 2); }
      SGB(0x008, 11);
    }
    SCHED();
  };
  BAR();
  STAMP(3, 1);
  rdA0(0); rdB(Bx, 0);
  for (int kb = 0; kb < 18; kb += 2) { step(Bx, By, kb, 0, 0); step(By, Bx, kb + 1, 1, 0); }
  STAMP(3, 2);
  {
    auto rp = [&](f32x4 a) {
      typedef short s16x2 __attribute__((ext_vector_type(2)));
      const s16x2 z = {0, 0};
      unsigned p0 = pk2(a[0], a[1]), p1 = pk2(a[2], a[3]);
      asm("" : "+v"(p0)); asm("" : "+v"(p1));
      s16x2 lo = __builtin_elementwise_max(__builtin_bit_cast(s16x2, p0), z), hi = __builtin_elementwise_max(__builtin_bit_cast(s16x2, p1), z);
      return u32x2{__builtin_bit_cast(unsigned, lo), __builtin_bit_cast(unsigned, hi)};
    };
#pragma unroll
    for (int p = 0; p < 4; p++) {
      int pos = 4 * p + w - rot; if (pos < 0) pos += 18;
      char* const hb = lds + vb + pos * 4096;
#pragma unroll
      for (int m = 0; m < 4; m++) {
        const u32x2 a = rp(acc[m][2 * p]), b = rp(acc[m][2 * p + 1]);
        *(LAS u32x4*)(hb + m * 1024) = u32x4{a[0], a[1], b[0], b[1]};
      }
    }
    int pos8 = 16 + (w >> 1) - rot; if (pos8 < 0) pos8 += 18;
    char* const h8 = lds + vb + pos8 * 4096 + (w & 1) * 8;
#pragma unroll
    for (int m = 0; m < 4; m++) *(LAS u32x2*)(h8 + m * 1024) = rp(acc[m][8]);
  }
  SCHED();
  STAMP(4, 0);
  step(Bx, By, 0, 0, 1);
  STAMP(4, 1);
  for (int kb = 1; kb < 17; kb += 2) { step(By, Bx, kb, 1, 0); step(Bx, By, kb + 1, 0, 0); }
  step(By, Bx, 17, 1, 3);
  STAMP(4, 2);
  char* const pb = ws + WS_PART + ((size_t)((tt * 72 + w) * 8 + fs) << 10) + (lane << 4);
#pragma unroll
  for (int j = 0; j < 9; j++)
#pragma unroll
    for (int mp = 0; mp < 2; mp++) {
      const f32x4 a = acc[2 * mp][j], b = acc[2 * mp + 1][j];
      *(u32x4*)(pb + (mp * 36 + 4 * j) * 8192) = u32x4{pk2(a[0], a[1]), pk2(a[2], a[3]), pk2(b[0], b[1]), pk2(b[2], b[3])};
    }
  STAMP(3, 3);
  STAMP(4, 3);
}

__global__ __launch_bounds__(576) void k5_out(char* __restrict__ ws, u16* __restrict__ out) {
  const int lane = threadIdx.x & 63, j = threadIdx.x >> 6, g = lane >> 4, c = lane & 15;
  const int xcd = blockIdx.x & 7, idx = blockIdx.x >> 3;
  const int nb = idx & 3, tb = xcd * 8 + (idx >> 2);
  STAMP(5, 0);
  const int u = (tb * 4 + nb) * 9 + j;
  const char* pp = ws + WS_PART + ((size_t)u << 13) + (lane << 4);
  u32x4 p[8];
#pragma unroll
  for (int i = 0; i < 8; i++) p[i] = *(const u32x4*)(pp + i * 1024);
  const float ra = ((const float*)(ws + WS_RSTD))[tb * 32 + c], rb = ((const float*)(ws + WS_RSTD))[tb * 32 + 16 + c];
  u32x2 xa = *(const u32x2*)(ws + WS_X2 + ((size_t)u << 10) + (lane << 3)), xb = *(const u32x2*)(ws + WS_X2 + ((size_t)u << 10) + 512 + (lane << 3));
  const int n = nb * 144 + 16 * j + 4 * g;
  const float* mp = (const float*)(ws + WS_MODP) + 5 * 576 + n;
  f32x4 gb = (*(const f32x4*)mp + *(const f32x4*)(mp + 3456)) + (*(const f32x4*)(mp + 2 * 3456) + *(const f32x4*)(mp + 3 * 3456));
#pragma unroll
  for (int e = 0; e < 4; e++) gb[e] = rbf(gb[e]);
  STAMP(5, 1);
  float z[8];
#pragma unroll
  for (int e = 0; e < 4; e++) {
    const float r = e < 2 ? ra : rb;
    z[2 * e] = r * (((bf_lo(p[0][e]) + bf_lo(p[1][e])) + (bf_lo(p[2][e]) + bf_lo(p[3][e]))) + ((bf_lo(p[4][e]) + bf_lo(p[5][e])) + (bf_lo(p[6][e]) + bf_lo(p[7][e]))));
    z[2 * e + 1] = r * (((bf_hi(p[0][e]) + bf_hi(p[1][e])) + (bf_hi(p[2][e]) + bf_hi(p[3][e]))) + ((bf_hi(p[4][e]) + bf_hi(p[5][e])) + (bf_hi(p[6][e]) + bf_hi(p[7][e]))));
  }
  u32x2 oa = {pk2(bf_lo(xa[0]) + gb[0] * z[0], bf_hi(xa[0]) + gb[1] * z[1]), pk2(bf_lo(xa[1]) + gb[2] * z[2], bf_hi(xa[1]) + gb[3] * z[3])};
  u32x2 ob = {pk2(bf_lo(xb[0]) + gb[0] * z[4], bf_hi(xb[0]) + gb[1] * z[5]), pk2(bf_lo(xb[1]) + gb[2] * z[6], bf_hi(xb[1]) + gb[3] * z[7])};
  STAMP(5, 2);
  *(u32x2*)(out + (size_t)(tb * 32 + c) * D + n) = oa;
  *(u32x2*)(out + (size_t)(tb * 32 + 16 + c) * D + n) = ob;
  STAMP(5, 3);
}

extern "C" void kernel_launch(void* const* d_in, const int* in_sizes, int n_in, void* d_out, int out_size, void* d_ws,
                              size_t ws_size, hipStream_t stream) {
  const u16* x = (const u16*)d_in[0];
  char* ws = (char*)d_ws;
  k0_ln_mod<<<256, 256, 0, stream>>>(x, (const u16*)d_in[8], (const u16*)d_in[9], (const u16*)d_in[1], (const u16*)d_in[2],
                                     (const u16*)d_in[3], (const u16*)d_in[4], ws);
  k1_seq<<<256, NT, 0, stream>>>((const u16*)d_in[1], (const u16*)d_in[2], (const u16*)d_in[6], ws);
  k2_hid<<<256, NT, 0, stream>>>(x, (const u16*)d_in[3], (const u16*)d_in[4], ws);
  km_mlp<<<256, NT, 0, stream>>>((const u16*)d_in[6], (const u16*)d_in[7], ws);
  k5_out<<<256, 576, 0, stream>>>(ws, (u16*)d_out);
}
```

```cpp
constexpr int CPF = 49152;
#include <hip/hip_runtime.h>
#include <stdint.h>
#include <type_traits>

typedef unsigned short u16;
typedef __bf16 bf16x8 __attribute__((ext_vector_type(8)));
typedef __bf16 bf16x2 __attribute__((ext_vector_type(2)));
typedef float f32x4 __attribute__((ext_vector_type(4)));
typedef float f32x2 __attribute__((ext_vector_type(2)));
typedef unsigned u32x4 __attribute__((ext_vector_type(4)));
typedef unsigned u32x2 __attribute__((ext_vector_type(2)));
typedef short s16x4 __attribute__((ext_vector_type(4)));
#define LAS __attribute__((address_space(3)))
#define DEVI __device__ __forceinline__
template <int N> using IC = std::integral_constant<int, N>;

constexpr int S = 2048, D = 576, F = 4608;
constexpr size_t WS_MODP = 0;
constexpr size_t WS_STAT = 192 << 10;
constexpr size_t WS_FLAG = 448 << 10;
constexpr size_t WS_XNT  = 1 << 20;
constexpr size_t WS_PQ   = 4 << 20;
constexpr size_t WS_X2   = 14 << 20;
constexpr size_t WS_PART = 20 << 20;
constexpr int FL_PQ = 0, FL_X2 = 16, FL_OUT = 48, FL_N = 112;
constexpr int SC1 = 16;

#define WAIT_V(n) asm volatile("s_waitcnt vmcnt(%0)" ::"n"(n) : "memory")
#define WAIT_L0() do { __builtin_amdgcn_s_waitcnt(0xC07F); asm volatile("" ::: "memory"); } while (0)
#define WAIT_L3() do { __builtin_amdgcn_s_waitcnt(0xC37F); asm volatile("" ::: "memory"); } while (0)
#define PIN(x) asm volatile("" : "+v"(x))
#define PINS(x) asm volatile("" : "+s"(x))
#define BAR() do { asm volatile("" ::: "memory"); __builtin_amdgcn_s_barrier(); asm volatile("" ::: "memory"); } while (0)
#define SCHED() __builtin_amdgcn_sched_barrier(0)
#define SGB(m, n) __builtin_amdgcn_sched_group_barrier(m, n, 0)

DEVI float bf_lo(unsigned v) { return __uint_as_float(v << 16); }
DEVI float bf_hi(unsigned v) { return __uint_as_float(v & 0xffff0000u); }
DEVI unsigned pk2(float a, float b) { bf16x2 p = {(__bf16)a, (__bf16)b}; return __builtin_bit_cast(unsigned, p); }
DEVI float rbf(float a) { return bf_lo(pk2(a, a) & 0xffffu); }
template <int CTRL> DEVI float dpp_add(float v) {
  int t = __builtin_amdgcn_update_dpp(0, __float_as_int(v), CTRL, 0xf, 0xf, true);
  return v + __int_as_float(t);
}
DEVI float x16_add(float v) {
  u32x2 r = __builtin_amdgcn_permlane16_swap(__float_as_uint(v), __float_as_uint(v), false, false);
  return __uint_as_float(r[0]) + __uint_as_float(r[1]);
}
DEVI float x32_add(float v) {
  u32x2 r = __builtin_amdgcn_permlane32_swap(__float_as_uint(v), __float_as_uint(v), false, false);
  return __uint_as_float(r[0]) + __uint_as_float(r[1]);
}
DEVI float row16_sum(float v) { v = dpp_add<0xB1>(v); v = dpp_add<0x4E>(v); v = dpp_add<0x141>(v); v = dpp_add<0x140>(v); return v; }
constexpr int VB = 7168, GB = 3072;
#define GL(v, g, o) "global_load_lds_dwordx4 %" #v ", %" #g " offset:%" #o "\n\t"
template <int LO, int GO> DEVI void dma8(const char* g, const unsigned* v, unsigned l) {
  asm volatile("s_add_u32 m0, %0, %2\n\ts_nop 0\n\t" GL(3, 1, 11) GL(4, 1, 12) GL(5, 1, 13) GL(6, 1, 14) GL(7, 1, 15) GL(8, 1, 16) GL(9, 1, 17) GL(10, 1, 18)
               ::"s"(l), "s"(g), "n"(LO + 4096 - GO), "v"(v[0]), "v"(v[1]), "v"(v[2]), "v"(v[3]), "v"(v[4]), "v"(v[5]), "v"(v[6]), "v"(v[7]),
               "n"(GO - 4096), "n"(GO - 3072), "n"(GO - 2048), "n"(GO - 1024), "n"(GO), "n"(GO + 1024), "n"(GO + 2048), "n"(GO + 3072) : "memory", "scc");
}
template <int LO, int GO> DEVI void dma2(const char* g, const unsigned* v, unsigned l) {
  asm volatile("s_add_u32 m0, %0, %2\n\ts_nop 0\n\t" GL(3, 1, 5) GL(4, 1, 6)
               ::"s"(l), "s"(g), "n"(LO + 4096 - GO), "v"(v[0]), "v"(v[1]), "n"(GO - 4096), "n"(GO - 3072) : "memory", "scc");
}
template <int LO, int GO> DEVI void dma1(const char* g, unsigned v, unsigned l) {
  asm volatile("s_add_u32 m0, %0, %2\n\ts_nop 0\n\t" GL(3, 1, 4) ::"s"(l), "s"(g), "n"(LO + 4096 - GO), "v"(v), "n"(GO - 4096) : "memory", "scc");
}
DEVI void dma8r(const char* g, const unsigned* v, unsigned la, unsigned so) {
  asm volatile("s_add_u32 m0, %0, %2\n\ts_nop 0\n\t" GL(3, 1, 11) GL(4, 1, 12) GL(5, 1, 13) GL(6, 1, 14) GL(7, 1, 15) GL(8, 1, 16) GL(9, 1, 17) GL(10, 1, 18)
               ::"s"(la), "s"(g), "s"(so), "v"(v[0]), "v"(v[1]), "v"(v[2]), "v"(v[3]), "v"(v[4]), "v"(v[5]), "v"(v[6]), "v"(v[7]),
               "n"(-4096), "n"(-3072), "n"(-2048), "n"(-1024), "n"(0), "n"(1024), "n"(2048), "n"(3072) : "memory", "scc");
}
DEVI void dma2r(const char* g, unsigned v, unsigned la, unsigned so) {
  asm volatile("s_add_u32 m0, %0, %2\n\ts_nop 0\n\t" GL(3, 1, 4) GL(3, 1, 5) ::"s"(la), "s"(g), "s"(so), "v"(v), "n"(-4096), "n"(-3072) : "memory", "scc");
}
template <int N, class Fn> DEVI void static_for(Fn&& f) {
  if constexpr (N > 0) { static_for<N - 1>(f); f(IC<N - 1>{}); }
}
DEVI unsigned touch(const char* p) { unsigned v; asm volatile("global_load_dword %0, %1, off" : "=v"(v) : "v"(p)); return v; }
DEVI auto rsrc(const char* base, unsigned n) { return __builtin_amdgcn_make_buffer_rsrc((char*)base, 0, n, 0x00020000); }
DEVI bf16x8 lds_frag(const char* p) { return *(const LAS bf16x8*)p; }
DEVI bf16x8 lds_frag_tr(const char* p, int off2) {
  s16x4 a = __builtin_amdgcn_ds_read_tr16_b64_v4i16((LAS s16x4*)p);
  s16x4 b = __builtin_amdgcn_ds_read_tr16_b64_v4i16((LAS s16x4*)(p + off2));
  return __builtin_bit_cast(bf16x8, __builtin_shufflevector(a, b, 0, 1, 2, 3, 4, 5, 6, 7));
}
DEVI f32x4 mfma(bf16x8 a, bf16x8 b, f32x4 c) { return __builtin_amdgcn_mfma_f32_16x16x32_bf16(a, b, c, 0, 0, 0); }

constexpr int TST = 5;
__global__ __launch_bounds__(320) void k0_ln_mod(const u16* __restrict__ x, const u16* __restrict__ cvec,
                                                 const u16* __restrict__ wmod, const u16* __restrict__ wsre, const u16* __restrict__ wsim,
                                                 const u16* __restrict__ wdre, const u16* __restrict__ wdim, char* __restrict__ ws) {
  __shared__ __attribute__((aligned(16))) char lds[8 * 1216];
  const int tid = threadIdx.x, lane = tid & 63, w = __builtin_amdgcn_readfirstlane(tid >> 6);
  if (blockIdx.x >= 256) {
    if (w == 4) return;
    const int b = blockIdx.x - 256, cg = b % 54, kq = b / 54;
    u32x4 wv[5]; u16 cv[5];
#pragma unroll
    for (int it = 0; it < 5; it++) {
      int r = (lane >> 1) + 32 * it; if (r > 143) r = 143;
      cv[it] = cvec[r + kq * 144];
    }
#pragma unroll
    for (int it = 0; it < 5; it++) {
      int r = (lane >> 1) + 32 * it; if (r > 143) r = 143;
      wv[it] = __builtin_nontemporal_load((const u32x4*)(wmod + (size_t)(r + kq * 144) * 3456 + cg * 64 + w * 16 + (lane & 1) * 8));
    }
    float a[8] = {0, 0, 0, 0, 0, 0, 0, 0};
#pragma unroll
    for (int it = 0; it < 5; it++) {
      const float c = bf_lo(cv[it]);
      float sl = rbf(c / (1.f + __expf(-c)));
      if (it == 4 && lane >= 32) sl = 0.f;
#pragma unroll
      for (int e = 0; e < 4; e++) { a[2 * e] += sl * bf_lo(wv[it][e]); a[2 * e + 1] += sl * bf_hi(wv[it][e]); }
    }
#pragma unroll
    for (int e = 0; e < 4; e++) {
      const u32x2 r = __builtin_amdgcn_permlane32_swap(__float_as_uint(a[e]), __float_as_uint(a[4 + e]), false, false);
      a[e] = __uint_as_float(r[0]) + __uint_as_float(r[1]);
    }
#pragma unroll
    for (int e = 0; e < 2; e++) {
      const u32x2 r = __builtin_amdgcn_permlane16_swap(__float_as_uint(a[e]), __float_as_uint(a[2 + e]), false, false);
      a[e] = dpp_add<0x122>(dpp_add<0x124>(dpp_add<0x128>(__uint_as_float(r[0]) + __uint_as_float(r[1]))));
    }
    if ((lane & 15) < 2) *(f32x2*)((float*)(ws + WS_MODP) + kq * 3456 + cg * 64 + w * 16 + (lane & 1) * 8 + (lane >> 4) * 2) = f32x2{a[0], a[1]};
    return;
  }
  const int b = blockIdx.x;
  if (w == 4) {
    __builtin_amdgcn_s_sleep(16);
    unsigned tv[6];
    const char* pc;
    asm volatile("s_getpc_b64 s[40:41]\n\ts_add_u32 s40, s40, kf_main@rel32@lo+4\n\ts_addc_u32 s41, s41, kf_main@rel32@hi+12\n\ts_mov_b64 %0, s[40:41]"
                 : "=s"(pc) :: "s40", "s41", "scc");
    int q = (b >> 3) * 13 + (lane < 13 ? lane : 12); if (q > CPF / 128 - 1) q = CPF / 128 - 1;
    tv[4] = touch(pc + q * 128);
    const int row = ((b & 7) >> 1) * 1024 + (b >> 3) * 32 + (lane & 31);
    const char* p = (const char*)(row < 2048 ? wsre : wsim) + (size_t)(row & 2047) * 4096 + (b & 1) * 2048;
#pragma unroll
    for (int i = 0; i < 4; i++) tv[i] = 2 * i < TST ? touch(p + (2 * i + 1 < TST ? 2 * i + (lane >> 5) : 2 * i) * 128) : 0;
    q = (b >> 1) * 41 + (lane < 41 ? lane : 40); if (q > 5183) q = 5183;
    tv[5] = touch((const char*)((b & 1) ? wdim : wdre) + q * 128);
    BAR();
    WAIT_V(0);
    asm volatile("" ::"v"(tv[0]), "v"(tv[1]), "v"(tv[2]), "v"(tv[3]), "v"(tv[4]), "v"(tv[5]));
    return;
  }
  const int tok = 8 * b + 2 * w + (lane >> 5), i = lane & 31;
  const unsigned* x32 = (const unsigned*)x + tok * 288 + i;
  unsigned xr[9];
#pragma unroll
  for (int n = 0; n < 9; n++) xr[n] = x32[32 * n];
  float v[18], s = 0.f;
#pragma unroll
  for (int n = 0; n < 9; n++) { v[2 * n] = bf_lo(xr[n]); v[2 * n + 1] = bf_hi(xr[n]); s += v[2 * n] + v[2 * n + 1]; }
  s = x16_add(row16_sum(s));
  const float mean = s * (1.f / D);
  float q = 0.f;
#pragma unroll
  for (int n = 0; n < 18; n++) { v[n] -= mean; q += v[n] * v[n]; }
  q = x16_add(row16_sum(q));
  const float rs = rsqrtf(q * (1.f / D) + 1e-5f);
  {
    char* row = lds + (2 * w + (lane >> 5)) * 1216 + i * 4;
#pragma unroll
    for (int n = 0; n < 9; n++) *(LAS unsigned*)(row + 128 * n) = pk2(v[2 * n] * rs, v[2 * n + 1] * rs);
  }
  if (b < FL_N && tid < 32) __hip_atomic_store((unsigned*)(ws + WS_FLAG) + b * 32 + tid, 0u, __ATOMIC_RELAXED, __HIP_MEMORY_SCOPE_AGENT);
  WAIT_L0(); BAR();
  const int grp = lane >> 4, c = lane & 15;
  for (int it = w; it < 9; it += 4) {
    const char* p = lds + (c >> 2) * 1216 + (64 * it + 16 * grp + 4 * (c & 3)) * 2;
    bf16x8 f = lds_frag_tr(p, 4 * 1216);
    const int J = 4 * it + grp, nb = J / 9, j = J - nb * 9;
    __builtin_amdgcn_raw_buffer_store_b128(__builtin_bit_cast(u32x4, f), rsrc(ws + WS_XNT, 4 * 64 * 9216),
                                           (((nb * 64 + (b >> 2)) * 9 + j) << 10) + (((b & 3) * 16 + c) << 4), 0, 0);
  }
  WAIT_V(0);
}

constexpr int NT = 384;
constexpr int K1_SLOT = 34816, K1_NS = 4, K1_NST = 16;
constexpr int K2_SLOT = 18432, K2_NST = 18;
constexpr int K2_PW = 10, TS0 = 2, TS1 = 8;
constexpr int KM_ACT = 73728, KM_SLOT = 20480;
constexpr int L_T1 = 8 * K2_SLOT, L_SM = KM_ACT + 4 * KM_SLOT, L_END = L_SM + 7552;
constexpr unsigned long long k1_ord(int rn) {
  unsigned long long o = 0;
  int n = 0, wm = 0, cd = TST + rn;
  auto put = [&](int s) { o |= (unsigned long long)s << (4 * n++); };
  while (wm < 3) put(wm++);
  while (cd < 16 || wm < TST) {
    if (cd < 16) { put(cd); cd += 4; }
    if (wm < TST) put(wm++);
  }
  for (int s = TST; s < 16; s++) if (((s - TST) & 3) != rn) put(s);
  return o;
}
#define K1_ORD(r) std::integral_constant<unsigned long long, k1_ord(r)>::value
DEVI void poll(const unsigned* p) {
#pragma nounroll
  for (int it = 0; it < (1 << 18); it++) {
    if (__builtin_amdgcn_ballot_w64(__hip_atomic_load(p, __ATOMIC_RELAXED, __HIP_MEMORY_SCOPE_AGENT) != 1u) == 0) break;
    __builtin_amdgcn_s_sleep(1);
  }
  asm volatile("" ::: "memory");
}
DEVI void spin(volatile LAS int* p) {
#pragma nounroll
  for (int it = 0; it < (1 << 22) && *p == 0; it++) __builtin_amdgcn_s_sleep(1);
  asm volatile("" ::: "memory");
}
DEVI int arrive(volatile LAS int* p, int lane) {
  int a = 0;
  if (lane == 0) a = __hip_atomic_fetch_add((LAS int*)p, 1, __ATOMIC_RELAXED, __HIP_MEMORY_SCOPE_WORKGROUP);
  return __builtin_amdgcn_readfirstlane(a);
}
template <class Fn> DEVI void handoff(volatile LAS int* sw, int lane, unsigned* mine, const unsigned* theirs, Fn&& meanwhile) {
  const bool last = arrive(sw, lane) == 3;
  if (last && lane == 0) __hip_atomic_store(mine, 1u, __ATOMIC_RELAXED, __HIP_MEMORY_SCOPE_AGENT);
  meanwhile(last);
  if (last) {
    poll(theirs);
    if (lane == 0) sw[1] = 1;
  } else spin(sw + 1);
}
extern "C" __global__ __launch_bounds__(NT) void kf_main(const u16* __restrict__ wsre, const u16* __restrict__ wsim, const u16* __restrict__ x,
                                              const u16* __restrict__ wdre, const u16* __restrict__ wdim, const u16* __restrict__ w1,
                                              const u16* __restrict__ w2, char* __restrict__ ws, u16* __restrict__ out) {
  __shared__ __attribute__((aligned(16))) char lds[L_END];
  float* const sca = (float*)(lds + L_T1);
  float* const ssa = sca + 144;
  float* const srs1 = ssa + 144;
  float* const ssb = (float*)(lds + L_SM);
  float* const sgb = ssb + 2 * D;
  float* const sga = sgb + D;
  volatile LAS int* const sw = (volatile LAS int*)(sga + 144);
  const int tid = threadIdx.x, lane = tid & 63, w = __builtin_amdgcn_readfirstlane(tid >> 6);
  const int xcd = blockIdx.x & 7, idx = blockIdx.x >> 3;
  const unsigned l0 = (unsigned)(size_t)(LAS char*)lds;
  unsigned* const flags = (unsigned*)(ws + WS_FLAG);
  const int ks = xcd & 1, nb = idx & 3, mb = (xcd >> 1) * 8 + (idx >> 2);
  const int n2 = idx & 3, tb = xcd * 8 + (idx >> 2), od = n2 & 1;
  const int fs = 2 * (xcd & 3) + (idx & 1), tt = 16 * (xcd >> 2) + (idx >> 1);
  const int rot = (((idx & ~1) + (xcd >> 2)) * 18) >> 5;
  const auto prs = rsrc(ws + WS_PART + (size_t)tt * (64 * 9216), 64 * 9216);
  const auto x2r = rsrc(ws + WS_X2 + (size_t)tt * KM_ACT + (fs >> 2) * 2048 + (fs & 3) * 64, KM_ACT - 2240);
  auto ldx2 = [&](int j, int h) {
    return __builtin_amdgcn_raw_buffer_load_b64(x2r, (lane >> 5) * 4096 + ((lane >> 2) & 3) * 256 + (lane & 3) * 16 + ((lane >> 4) & 1) * 8 + h * 1024, j * 8192, SC1);
  };
  auto finish = [&](const u32x4* p, u32x2 xa, u32x2 xb, int j) {
    const int tk = (fs >> 2) * 32 + (fs & 3) * 4 + (lane & 3);
    const f32x4 gb = *(const LAS f32x4*)(sgb + 64 * j + (lane >> 2) * 4);
    float z[8];
#pragma unroll
    for (int e = 0; e < 4; e++) {
      z[2 * e] = ((bf_lo(p[0][e]) + bf_lo(p[1][e])) + (bf_lo(p[2][e]) + bf_lo(p[3][e]))) + ((bf_lo(p[4][e]) + bf_lo(p[5][e])) + (bf_lo(p[6][e]) + bf_lo(p[7][e])));
      z[2 * e + 1] = ((bf_hi(p[0][e]) + bf_hi(p[1][e])) + (bf_hi(p[2][e]) + bf_hi(p[3][e]))) + ((bf_hi(p[4][e]) + bf_hi(p[5][e])) + (bf_hi(p[6][e]) + bf_hi(p[7][e])));
    }
    u16* const o = out + (size_t)(tt * 64 + tk) * D + 64 * j + (lane >> 2) * 4;
    *(u32x2*)o = u32x2{pk2(bf_lo(xa[0]) + gb[0] * z[0], bf_hi(xa[0]) + gb[1] * z[1]), pk2(bf_lo(xa[1]) + gb[2] * z[2], bf_hi(xa[1]) + gb[3] * z[3])};
    *(u32x2*)(o + 16 * D) = u32x2{pk2(bf_lo(xb[0]) + gb[0] * z[4], bf_hi(xb[0]) + gb[1] * z[5]), pk2(bf_lo(xb[1]) + gb[2] * z[6], bf_hi(xb[1]) + gb[3] * z[7])};
  };
  auto gather = [&](u32x4* p, int j) {
#pragma unroll
    for (int i = 0; i < 8; i++) p[i] = __builtin_amdgcn_raw_buffer_load_b128(prs, (lane << 4) + j * 1024, (fs * 8 + i) * 9216, SC1);
  };
  const int g = lane >> 4, c = lane & 15;
  if (w >= 4) {
    const int li = w - 4;
    unsigned vo[8], wv[9];
    unsigned long long ord = K1_ORD(0);
    ord = nb == 1 ? K1_ORD(1) : ord; ord = nb == 2 ? K1_ORD(2) : ord; ord = nb == 3 ? K1_ORD(3) : ord;
    auto load = [&](auto LI) {
      const char* base = LI ? ws + WS_XNT + (size_t)(nb * 64 + ks * 32) * 9216 - GB
                            : (const char*)(mb >= 16 ? wsim : wsre) + (size_t)(mb & 15) * 128 * 4096 + ks * 2048 - GB;
      unsigned v2[8];
#pragma unroll
      for (int p = 0; p < 8; p++) {
        vo[p] = LI ? (lane << 4) + VB : (lane >> 3) * 4096 + (((lane & 7) ^ (lane >> 3)) << 4) + p * (32768 - 1024) + VB;
        v2[p] = vo[p] + (LI ? 8192 : 8 * 32768);
      }
      const unsigned v3 = vo[0] + 16384, la = l0 + 4096 + LI * 16384, lb = la + 8192, lc = la + 16384;
      const char* g = base + ((unsigned)ord & 15u) * (LI ? 18432u : 128u);
      unsigned so = 0;
#pragma nounroll
      for (int i = 0; i < K1_NST; i++) {
        dma8r(g, vo, la, so);
        PINS(ord);
        ord >>= 4;
        const char* gn = base + ((unsigned)ord & 15u) * (LI ? 18432u : 128u);
        unsigned sn = so + K1_SLOT; if (sn == K1_NS * K1_SLOT) sn = 0;
        PINS(gn); PINS(sn);
        dma8r(g, v2, lb, so);
        if (LI) dma2r(g, v3, lc, so);
        g = gn; so = sn;
        if (i == 1) {
#pragma unroll
          for (int p = 0; p < 9; p++) {
            const int P = 9 * LI + p, a = P >> 1, j = a < 4 ? a + od : a == 4 ? 8 - 8 * od : a - 1 + od;
            wv[p] = (16 * j + 8 * (P & 1) + (lane >> 3)) * 1152 + (((lane & 7) ^ (lane >> 3)) << 4) + VB - 1024 * (p & 7);
            PIN(wv[p]);
          }
        }
        if (i >= 2) { WAIT_V(LI ? 36 : 32); BAR(); }
      }
    };
    if (li) load(IC<1>{}); else load(IC<0>{});
    const char* const gre = (const char*)wdre + n2 * 144 * 1152 - GB;
    const char* const gim = (const char*)wdim + n2 * 144 * 1152 - GB;
    const unsigned lw = l0 + li * 9216;
    auto wd = [&](auto SI) {
      constexpr int s = SI, i = s % 9, LO = ((s + 1) & 7) * K2_SLOT, GO = (i & 7) * 128;
      const char* gg = (s < 9 ? gre : gim) + (i & 8) * 128;
      dma8<LO, GO>(gg, wv, lw); dma1<LO + 8192, GO>(gg, wv[8], lw);
    };
    WAIT_V(16); BAR();
    wd(IC<0>{}); wd(IC<1>{});
    WAIT_V(18); BAR();
    wd(IC<2>{}); wd(IC<3>{});
    BAR();
    wd(IC<4>{}); wd(IC<5>{}); wd(IC<6>{});
    WAIT_V(54);
    wd(IC<7>{});
    const unsigned swz = ((lane & 7) ^ (((lane >> 4) & 3) << 1)) << 4;
#pragma unroll
    for (int p = 0; p < 8; p++) { vo[p] = ((lane >> 3) + (p & 1) * 8) * 9216 + swz + (p >> 1) * 128 + VB - 1024 * p; PIN(vo[p]); }
    const char* w1b = (const char*)w1 + fs * 1152 + li * 16 * 9216 - GB;
    const char* w2b = (const char*)w2 + (size_t)fs * 576 * 1152 - GB;
    const unsigned lr = l0 + KM_ACT + li * 10240;
    int k = rot;
    const char* src = w1b + k * (32 * 9216);
    auto h0 = [&](auto par) { dma8<par * 2 * KM_SLOT, 0>(src, vo, lr); dma2<par * 2 * KM_SLOT + 8192, 512>(src, vo, lr); };
    auto h1 = [&](auto par) { dma8<(par * 2 + 1) * KM_SLOT, 640>(src, vo, lr); };
    auto nx1 = [&]() { k++; src += 32 * 9216; if (k == 18) { k = 0; src = w1b; } PINS(src); };
    const IC<0> P0; const IC<1> P1; const IC<2> P2; const IC<3> P3;
    unsigned wt[5];
    {
      unsigned t = 0;
#pragma unroll
      for (int e = 0; e < 4; e++) {
        int kq = 4 * (lane & 7) + e + 17 + rot; kq -= kq >= 36 ? 36 : kq >= 18 ? 18 : 0;
        t |= (unsigned)(kq < 16 ? 8 * (kq >> 2) + 4 * li + (kq & 3) : (2 * kq + li) & 63) << (8 * e);
      }
#pragma unroll
      for (int i = 0; i < 5; i++) { wt[i] = __builtin_amdgcn_readlane(t, i); PINS(wt[i]); }
    }
    static_for<K2_NST>([&](auto SI) {
      constexpr int s = SI;
      WAIT_V(9 * (s == 0 ? 7 : s < 12 ? 6 : 17 - s) + (s == 14 ? 10 : s > 14 ? 18 : 0));
      BAR();
      if constexpr (s >= 1 && s + 7 < K2_NST) wd(IC<s + 7>{});
      if (s == 13) h0(P0);
      if (s == 14) { h1(P0); nx1(); }
    });
    BAR();
    h0(P1); h1(P1); nx1();
    WAIT_V(26); BAR();
#pragma nounroll
    for (int s = 2; s < 18; s += 2) {
      WAIT_V(18); BAR(); h0(P0); WAIT_V(18); BAR(); h1(P0); nx1();
      WAIT_V(18); BAR(); h0(P1); WAIT_V(18); BAR(); h1(P1); nx1();
    }
#pragma unroll
    for (int p = 0; p < 8; p++) { vo[p] = ((lane >> 3) + (p & 1) * 8) * 1152 + swz + (p >> 1) * 128 + VB - 1024 * p; PIN(vo[p]); }
    auto nx2 = [&](unsigned t, int e) { src = w2b + ((t >> (8 * e)) & 255u) * (16u * 1152u); PINS(src); };
    auto sa = [&](auto SL, auto W) { WAIT_V(W.value); BAR(); dma8<SL * KM_SLOT, 0>(src, vo, lr); dma2<SL * KM_SLOT + 8192, 512>(src, vo, lr); };
    auto sb = [&](auto SL, auto W) { WAIT_V(W.value); BAR(); dma8<SL * KM_SLOT, 0>(src, vo, lr); };
    const IC<16> W16; const IC<18> W18; const IC<20> W20;
    auto rotw = [&]() { const unsigned t = wt[1]; wt[1] = wt[2]; wt[2] = wt[3]; wt[3] = wt[4]; wt[4] = t; PINS(wt[1]); };
    nx2(wt[0], 1);
    sa(P0, W18); nx2(wt[0], 2); sa(P1, W18); nx2(wt[0], 3);
#pragma nounroll
    for (int s = 0; s < 4; s++) {
      sa(P2, W20); nx2(wt[1], 0); sa(P3, W20); nx2(wt[1], 1); sa(P0, W20); nx2(wt[1], 2); sa(P1, W20);
      if (s == 3) w2b += 640;
      nx2(wt[1], 3); rotw();
    }
    sb(P2, W20); nx2(wt[0], 2); sb(P3, W18); nx2(wt[0], 3);
#pragma nounroll
    for (int s = 0; s < 4; s++) { sb(P0, W16); nx2(wt[1], 0); sb(P1, W16); nx2(wt[1], 1); sb(P2, W16); nx2(wt[1], 2); sb(P3, W16); nx2(wt[1], 3); rotw(); }
    WAIT_V(16); BAR(); WAIT_V(8); BAR(); WAIT_V(0); BAR();
    auto tail = [&](auto J0, auto NJ) {
      u32x2 xa[NJ], xb[NJ]; u32x4 p[NJ][8];
#pragma unroll
      for (int j = 0; j < NJ; j++) { xa[j] = ldx2(J0 + j, 0); xb[j] = ldx2(J0 + j, 1); }
      poll(flags + (FL_OUT + 2 * tt) * 32 + (lane & 7));
#pragma unroll
      for (int j = 0; j < NJ; j++) gather(p[j], J0 + j);
#pragma unroll
      for (int j = 0; j < NJ; j++) finish(p[j], xa[j], xb[j], J0 + j);
    };
    if (li) tail(IC<3>{}, IC<2>{}); else tail(IC<0>{}, IC<3>{});
    return;
  }
  const int mh = w >> 1, kh = w & 1;
  const int tok = tb * 32 + mh * 16 + c;
  const int j0 = 4 * kh + od, j4 = 8 - 8 * od;
  u32x2 xr[5];
  f32x4 tq[8];
  auto ldtq = [&]() {
#pragma unroll
    for (int i = 0; i < 8; i++) tq[i] = ((const f32x4*)(ws + WS_MODP))[3 * 144 + (i & 3) * 864 + ((i >> 2) * 256 + tid < 432 ? (i >> 2) * 256 + tid : 431)];
  };
  {
    float mq[12];
    {
      const float* modp = (const float*)(ws + WS_MODP) + (tid < 144 ? tid : 0);
#pragma unroll
      for (int i = 0; i < 8; i++) mq[i] = modp[nb * 144 + (i >> 1) * 3456 + (i & 1) * 576];
#pragma unroll
      for (int i = 0; i < 4; i++) mq[8 + i] = modp[2 * 576 + n2 * 144 + i * 3456];
      if (tid < 16) sw[tid] = 0;
    }
    const int vb = (64 * mh + c) * 128 + ((g ^ (c & 7)) << 4);
    const int va = 16384 + kh * 5120 + (lane << 4);
    unsigned pbb[2][2], pab[2];
#pragma unroll
    for (int h = 0; h < 2; h++) {
      pbb[h][0] = l0 + vb + h * 2 * K1_SLOT; pbb[h][1] = l0 + (vb ^ 64) + h * 2 * K1_SLOT;
      pab[h] = l0 + va + h * 2 * K1_SLOT;
      PIN(pbb[h][0]); PIN(pbb[h][1]); PIN(pab[h]);
    }
    auto run = [&](auto NH) {
      constexpr int NR = NH ? 8 : 9;
      bf16x8 B0[4], A0[5], B1[4], A1[5];
      f32x4 acc[4][5];
#pragma unroll
      for (int m = 0; m < 4; m++)
#pragma unroll
        for (int j = 0; j < 5; j++) { acc[m][j] = f32x4{0, 0, 0, 0}; PIN(acc[m][j]); }
      if (NH) {
        u32x4 one = {0x3f803f80u, 0x3f803f80u, 0x3f803f80u, 0x3f803f80u};
        PIN(one);
        A0[4] = A1[4] = __builtin_bit_cast(bf16x8, one);
      }
      auto rd = [&](bf16x8* B, bf16x8* A, int sl, int hk) {
        const LAS char* pb = (const LAS char*)(size_t)pbb[sl >> 1][hk] + (sl & 1) * K1_SLOT;
        const LAS char* pa = (const LAS char*)(size_t)pab[sl >> 1] + (sl & 1) * K1_SLOT + hk * 9216;
        B[0] = *(const LAS bf16x8*)pb; A[0] = *(const LAS bf16x8*)pa;
#pragma unroll
        for (int m = 1; m < 4; m++) B[m] = *(const LAS bf16x8*)(pb + m * 2048);
#pragma unroll
        for (int j = 1; j < (NH ? 4 : 5); j++) A[j] = *(const LAS bf16x8*)(pa + j * 1024);
      };
      auto mm = [&](bf16x8* B, bf16x8* A) {
#pragma unroll
        for (int j = 0; j < 5; j++)
#pragma unroll
          for (int m = 0; m < 4; m++) acc[m][j] = mfma(A[j], B[m], acc[m][j]);
      };
      auto pat = [&]() {
#pragma unroll
        for (int i = 0; i < NR; i++) { SGB(0x008, 1); SGB(0x100, 1); }
        SGB(0x008, 20 - NR);
      };
      auto stage = [&](int sl, bool last) {
        SCHED();
        rd(B1, A1, sl, 1); mm(B0, A0); pat();
        SCHED();
        WAIT_L0(); BAR();
        SCHED();
        if (!last) { rd(B0, A0, (sl + 1) & 3, 0); mm(B1, A1); pat(); } else mm(B1, A1);
      };
      if (tid < 144) {
        ssa[tid] = rbf((mq[0] + mq[2]) + (mq[4] + mq[6])); sca[tid] = rbf(1.f + rbf((mq[1] + mq[3]) + (mq[5] + mq[7])));
        sga[tid] = rbf((mq[8] + mq[9]) + (mq[10] + mq[11]));
      }
      WAIT_L0();
      BAR();
      rd(B0, A0, 0, 0);
#pragma nounroll
      for (int s = 0; s < K1_NST - 4; s += 4) { stage(0, false); stage(1, false); stage(2, false); stage(3, false); }
      stage(0, false); stage(1, false); stage(2, false); stage(3, true);
      SCHED();
      if (NH == 1) {
        if (g == 0) {
#pragma unroll
          for (int m = 0; m < 4; m++) srs1[64 * mh + 16 * m + c] = acc[m][4][0];
        }
        WAIT_L0();
        if (lane == 0) sw[mh] = 1;
      } else spin(sw + mh);
      asm volatile("" ::: "memory");
      float rsum[4];
#pragma unroll
      for (int m = 0; m < 4; m++) rsum[m] = srs1[64 * mh + 16 * m + c];
      const float sgn = mb >= 16 ? -1.f : 1.f;
      const auto pq = rsrc(ws + WS_PQ + (size_t)((mb & 15) * 4 + 2 * mh) * 147456, 2 * 147456);
#pragma unroll
      for (int j = 0; j < 5; j++) {
        if (j == 4 && NH) break;
        const int jj = 5 * NH + j;
        f32x4 ca = *(const LAS f32x4*)(sca + 16 * jj + 4 * g), sa = *(const LAS f32x4*)(ssa + 16 * jj + 4 * g);
        ca *= sgn; sa *= sgn;
        const int Jp = (mb >> 4) * 36 + 9 * nb + jj;
#pragma unroll
        for (int mp = 0; mp < 2; mp++) {
          f32x4 p0 = acc[2 * mp][j] * ca + rsum[2 * mp] * sa, p1 = acc[2 * mp + 1][j] * ca + rsum[2 * mp + 1] * sa;
          u32x2 lo = __builtin_amdgcn_permlane16_swap(pk2(p0[0], p0[1]), pk2(p1[0], p1[1]), false, false);
          u32x2 hi = __builtin_amdgcn_permlane16_swap(pk2(p0[2], p0[3]), pk2(p1[2], p1[3]), false, false);
          __builtin_amdgcn_raw_buffer_store_b128(u32x4{lo[0], hi[0], lo[1], hi[1]}, pq, ((g & 1) << 10) + (((g >> 1) * 16 + c) << 4) +
                                                 ((mp * 36 + (Jp >> 1)) << 12) + (ks << 11) + ((Jp & 1) << 9), 0, SC1);
        }
      }
    };
    if (kh) run(IC<1>{}); else run(IC<0>{});
  }
  {
    unsigned pa[3];
#pragma unroll
    for (int i = 0; i < 3; i++) { pa[i] = l0 + c * 128 + (((4 * kh + g) ^ (c & 7)) << 4) + i * 3 * K2_SLOT; PIN(pa[i]); }
    f32x4 acc[9];
    bf16x8 B[K2_NST][2], A0[5], A1[4];
    const auto pq = rsrc(ws + WS_PQ + (size_t)tb * 147456, 147456);
    const unsigned vq = (lane << 4) + mh * 1024 + kh * 4096;
    auto ldb = [&](int s) {
#pragma unroll
      for (int h = 0; h < 2; h++) B[s][h] = __builtin_bit_cast(bf16x8, __builtin_amdgcn_raw_buffer_load_b128(pq, vq + h * 2048, s * 8192, SC1));
    };
    auto rda = [&](bf16x8* A, int s, int a0, int n) {
      const int p = (s + 1) & 7;
      const LAS char* b = (const LAS char*)(size_t)pa[p / 3] + (p % 3) * K2_SLOT + a0 * 2048;
#pragma unroll
      for (int j = 0; j < n; j++) A[j] = *(const LAS bf16x8*)(b + j * 2048);
    };
    WAIT_V(0);
    handoff(sw + 6, lane, flags + (FL_PQ + (mb & 15)) * 32 + (mb >> 4) * 8 + nb * 2 + ks, flags + (FL_PQ + (tb >> 2)) * 32 + (lane & 15), [&](bool) {
#pragma unroll
      for (int jj = 0; jj < 5; jj++) xr[jj] = *(const u32x2*)(x + (size_t)tok * D + n2 * 144 + 16 * (jj < 4 ? j0 + jj : j4) + 4 * g);
      ldtq();
    });
#pragma unroll
    for (int s = 0; s < K2_PW; s++) { ldb(s); SCHED(); }
    BAR();
    rda(A0, 0, 0, 5);
    auto table = [&](int r) {
      const int e = r * 256 + tid < 432 ? r * 256 + tid : 431;
      f32x4 t = (tq[4 * r] + tq[4 * r + 1]) + (tq[4 * r + 2] + tq[4 * r + 3]);
#pragma unroll
      for (int i = 0; i < 4; i++) { t[i] = rbf(t[i]); if (e >= 144 && e < 288) t[i] = rbf(1.f + t[i]); }
      *(LAS f32x4*)(ssb + 4 * e) = t;
    };
    f32x4 ga[5], xv[5];
    static_for<K2_NST>([&](auto SI) {
      constexpr int s = SI;
      const f32x4 zero = {0, 0, 0, 0};
      SCHED();
      rda(A1, s, 5, 4);
#pragma unroll
      for (int h = 0; h < 2; h++)
#pragma unroll
        for (int j = 0; j < 5; j++) acc[j] = mfma(A0[j], B[s][h], s == 0 && h == 0 ? zero : acc[j]);
#pragma unroll
      for (int i = 0; i < 4; i++) { SGB(0x008, 1); SGB(0x100, 1); }
      SGB(0x008, 6);
      SCHED();
      if (s + 1 < K2_NST) {
        WAIT_L0(); BAR();
        if (s == TS1 + 1) {
#pragma unroll
          for (int jj = 0; jj < 5; jj++) PIN(ga[jj]);
        }
        SCHED();
        rda(A0, s + 1, 0, 5);
        if (s + K2_PW < K2_NST) ldb(s + K2_PW);
        if (s == TS0 || s == TS0 + 1) table(s - TS0);
        if (s == TS1) {
#pragma unroll
          for (int jj = 0; jj < 5; jj++) {
            ga[jj] = *(const LAS f32x4*)(sga + 16 * (jj < 4 ? j0 + jj : j4) + 4 * g);
            xv[jj] = f32x4{bf_lo(xr[jj][0]), bf_hi(xr[jj][0]), bf_lo(xr[jj][1]), bf_hi(xr[jj][1])};
          }
        }
      }
#pragma unroll
      for (int h = 0; h < 2; h++)
#pragma unroll
        for (int j = 0; j < 4; j++) acc[5 + j] = mfma(A1[j], B[s][h], s == 0 && h == 0 ? zero : acc[5 + j]);
      if (s + 1 < K2_NST) {
#pragma unroll
        for (int i = 0; i < 5; i++) { SGB(0x008, 1); SGB(0x100, 1); }
        if (s + K2_PW < K2_NST) { SGB(0x008, 1); SGB(0x020, 1); SGB(0x008, 1); SGB(0x020, 1); SGB(0x008, 1); }
        else SGB(0x008, 3);
        if (s == TS0 || s == TS0 + 1) SGB(0x002, 40);
        if (s == TS1) {
          SGB(0x100, 5); SGB(0x002, 20);
#pragma unroll
          for (int jj = 0; jj < 5; jj++) PIN(xv[jj]);
        }
      }
    });
    SCHED();
    {
      char* xb = lds + 3 * K2_SLOT + mh * 9216 + (lane << 4);
      if (kh == 0) {
#pragma unroll
        for (int j = 5; j < 9; j++) *(LAS f32x4*)(xb + j * 1024) = acc[j];
      } else {
#pragma unroll
        for (int j = 0; j < 5; j++) *(LAS f32x4*)(xb + j * 1024) = acc[j];
      }
      WAIT_L0(); BAR();
      if (kh == 0) {
#pragma unroll
        for (int j = 0; j < 5; j++) acc[j] += *(const LAS f32x4*)(xb + j * 1024);
      } else {
#pragma unroll
        for (int j = 0; j < 4; j++) acc[j] = acc[5 + j] + *(const LAS f32x4*)(xb + (5 + j) * 1024);
      }
    }
    const int nj = 5 - kh;
    float sum = 0.f;
    u32x2 o1[5];
    const auto xp = rsrc(ws + WS_X2 + (size_t)(tb >> 1) * KM_ACT, KM_ACT);
    const unsigned v2 = (((tb & 1) * 2 + mh) << 10) + (lane << 4);
#pragma unroll
    for (int jj = 0; jj < 5; jj++) {
      if (jj < nj) {
        const int j = jj < 4 ? j0 + jj : j4;
        f32x4 x2 = xv[jj] + ga[jj] * acc[jj];
        o1[jj] = u32x2{pk2(x2[0], x2[1]), pk2(x2[2], x2[3])};
        const int J = 9 * n2 + j;
        if (jj & 1) __builtin_amdgcn_raw_buffer_store_b128(u32x4{o1[jj - 1][0], o1[jj - 1][1], o1[jj][0], o1[jj][1]}, xp, v2 + ((J >> 1) << 12), 0, SC1);
        else if (jj == 4) __builtin_amdgcn_raw_buffer_store_b64(o1[4], xp, v2, ((J >> 1) << 12) + od * 8, SC1);
        x2 = f32x4{bf_lo(o1[jj][0]), bf_hi(o1[jj][0]), bf_lo(o1[jj][1]), bf_hi(o1[jj][1])};
        acc[jj] = x2;
        sum += (x2[0] + x2[1]) + (x2[2] + x2[3]);
      }
    }
    sum = x32_add(x16_add(sum));
    const float lm = sum * (kh ? 1.f / 64 : 1.f / 80);
    float m2 = 0.f;
#pragma unroll
    for (int jj = 0; jj < 5; jj++) {
      if (jj < nj) {
        f32x4 d = acc[jj] - lm;
        m2 += (d[0] * d[0] + d[1] * d[1]) + (d[2] * d[2] + d[3] * d[3]);
      }
    }
    m2 = x32_add(x16_add(m2));
    if (g == 0) __builtin_amdgcn_raw_buffer_store_b64(u32x2{__float_as_uint(sum), __float_as_uint(m2)}, rsrc(ws + WS_STAT, 8 * S * 8), ((n2 * 2 + kh) * S + tok) << 3, 0, SC1);
  }
  WAIT_V(0);
  f32x4 acc[4][9];
  auto clr = [&](int m0, int m1) {
#pragma unroll
    for (int m = m0; m < m1; m++)
#pragma unroll
      for (int j = 0; j < 9; j++) { acc[m][j] = f32x4{0, 0, 0, 0}; PIN(acc[m][j]); }
  };
  handoff(sw + 8, lane, flags + (FL_X2 + (tb >> 1)) * 32 + (tb & 1) * 4 + n2, flags + (FL_X2 + tt) * 32 + (lane & 7), [&](bool) { clr(0, 3); });
  const int vb = lane << 4;
  {
    u32x2 p[8]; u32x4 xq[18]; f32x4 tb4[3][4];
    const auto sr = rsrc(ws + WS_STAT, 8 * S * 8);
    const auto xp = rsrc(ws + WS_X2 + (size_t)tt * KM_ACT, KM_ACT);
#pragma unroll
    for (int i = 0; i < 8; i++) p[i] = __builtin_amdgcn_raw_buffer_load_b64(sr, (tt * 64 + w * 16 + c) << 3, i * S * 8, SC1);
#pragma unroll
    for (int q = 0; q < 18; q++) { SCHED(); xq[q] = __builtin_amdgcn_raw_buffer_load_b128(xp, vb + w * 1024, q * 4096, SC1); }
    SCHED();
    unsigned ta = l0 + L_SM + g * 16;
    PIN(ta);
    auto rdt = [&](int q) {
      const LAS f32x4* t = (const LAS f32x4*)(size_t)ta + q * 8;
#pragma unroll
      for (int h = 0; h < 2; h++) { tb4[q % 3][2 * h] = t[4 * h]; tb4[q % 3][2 * h + 1] = t[D / 4 + 4 * h]; }
    };
    rdt(0);
    SCHED();
    float sum = 0.f;
#pragma unroll
    for (int i = 0; i < 8; i++) sum += __uint_as_float(p[i][0]);
    const float mean = sum * (1.f / D);
    float m2 = 0.f;
#pragma unroll
    for (int i = 0; i < 8; i++) {
      const float n = (i & 1) ? 64.f : 80.f, d = __uint_as_float(p[i][0]) * ((i & 1) ? 1.f / 64 : 1.f / 80) - mean;
      m2 += __uint_as_float(p[i][1]) + n * d * d;
    }
    const float rs = rsqrtf(m2 * (1.f / D) + 1e-5f), nm = -mean * rs;
    SCHED();
    rdt(1);
#pragma unroll
    for (int q = 0; q < 18; q++) {
      SCHED();
      if (q + 2 < 18) rdt(q + 2);
      SCHED();
      u32x4 o;
#pragma unroll
      for (int h = 0; h < 2; h++) {
        const f32x4 v = {bf_lo(xq[q][2 * h]), bf_hi(xq[q][2 * h]), bf_lo(xq[q][2 * h + 1]), bf_hi(xq[q][2 * h + 1])};
        const f32x4 r = (v * rs + nm) * tb4[q % 3][2 * h + 1] + tb4[q % 3][2 * h];
        o[2 * h] = pk2(r[0], r[1]); o[2 * h + 1] = pk2(r[2], r[3]);
      }
      *(LAS u32x4*)(lds + vb + w * 1024 + q * 4096) = o;
    }
    SCHED();
    clr(3, 4);
  }
  const int va = KM_ACT + (4 * g + (c >> 2)) * 128 + ((w ^ ((2 * g + (c >> 3)) & 3)) << 5) + (c & 3) * 8;
  bf16x8 Bx[4], By[4], A0[5], A1[5];
  unsigned vap[2] = {l0 + va, l0 + va + 2 * KM_SLOT};
  PIN(vap[0]); PIN(vap[1]);
  auto rdA = [&](bf16x8* A, int n, int sl) {
    const char* b = (const char*)(const LAS char*)(size_t)vap[sl >> 1] + (sl & 1) * KM_SLOT;
#pragma unroll
    for (int j = 0; j < n; j++) A[j] = lds_frag_tr(b + j * 2048, 10240);
  };
  auto rdB = [&](bf16x8* B, int kb) {
#pragma unroll
    for (int m = 0; m < 4; m++) B[m] = lds_frag(lds + vb + kb * 4096 + m * 1024);
  };
  int kb = rot;
  auto step = [&](bf16x8* Bc, bf16x8* Bn, int par) {
    WAIT_L0(); BAR();
    SCHED();
    kb = kb == 17 ? 0 : kb + 1;
    rdA(A1, 4, par * 2 + 1);
#pragma unroll
    for (int j = 0; j < 5; j++)
#pragma unroll
      for (int m = 0; m < 4; m++) acc[m][j] = mfma(A0[j], Bc[m], acc[m][j]);
#pragma unroll
    for (int i = 0; i < 8; i++) { SGB(0x008, 1); SGB(0x100, 1); }
    SGB(0x008, 12);
    SCHED();
    WAIT_L0(); BAR();
    SCHED();
    rdA(A0, 5, (par ^ 1) * 2);
    rdB(Bn, kb);
#pragma unroll
    for (int j = 0; j < 4; j++)
#pragma unroll
      for (int m = 0; m < 4; m++) acc[m][5 + j] = mfma(A1[j], Bc[m], acc[m][5 + j]);
#pragma unroll
    for (int i = 0; i < 7; i++) { SGB(0x008, 1); SGB(0x100, 2); }
    SGB(0x008, 9);
    SCHED();
  };
  WAIT_L0(); BAR();
  rdA(A0, 5, 0); rdB(Bx, kb);
#pragma nounroll
  for (int n = 0; n < 9; n++) { step(Bx, By, 0); step(By, Bx, 1); }
  {
    auto rp = [&](f32x4 a) {
      typedef short s16x2 __attribute__((ext_vector_type(2)));
      const s16x2 z = {0, 0};
      unsigned p0 = pk2(a[0], a[1]), p1 = pk2(a[2], a[3]);
      asm("" : "+v"(p0)); asm("" : "+v"(p1));
      s16x2 lo = __builtin_elementwise_max(__builtin_bit_cast(s16x2, p0), z), hi = __builtin_elementwise_max(__builtin_bit_cast(s16x2, p1), z);
      return u32x2{__builtin_bit_cast(unsigned, lo), __builtin_bit_cast(unsigned, hi)};
    };
#pragma unroll
    for (int p = 0; p < 4; p++) {
      int pos = 4 * p + w - rot; if (pos < 0) pos += 18;
      char* const hb = lds + vb + pos * 4096;
#pragma unroll
      for (int m = 0; m < 4; m++) {
        const u32x2 a = rp(acc[m][2 * p]), b = rp(acc[m][2 * p + 1]);
        *(LAS u32x4*)(hb + m * 1024) = u32x4{a[0], a[1], b[0], b[1]};
      }
    }
    int pos8 = 16 + (w >> 1) - rot; if (pos8 < 0) pos8 += 18;
    char* const h8 = lds + vb + pos8 * 4096 + (w & 1) * 8;
#pragma unroll
    for (int m = 0; m < 4; m++) *(LAS u32x2*)(h8 + m * 1024) = rp(acc[m][8]);
  }
  SCHED();
  const unsigned pv = (c >> 2) * 73728 + w * 256 + g * 64 + (c & 3) * 16 + fs * 9216;
  auto put = [&](int j, int mp) {
    const f32x4 a = acc[2 * mp][j], b = acc[2 * mp + 1][j];
    __builtin_amdgcn_raw_buffer_store_b128(u32x4{pk2(a[0], a[1]), pk2(a[2], a[3]), pk2(b[0], b[1]), pk2(b[2], b[3])}, prs, pv + mp * 4 * 73728 + j * 1024, 0, SC1);
  };
  u32x2 xa, xb;
  auto step2 = [&](auto N, int kn) {
    constexpr int NC = N < 18 ? 5 : 4, NN = N < 17 ? 5 : N < 35 ? 4 : 0, J0 = N < 18 ? 0 : 5, PUT = N >= 18 && N < 28 ? N - 18 : -1;
    constexpr bool FIRST = N == 0 || N == 18;
    bf16x8* const Ac = N & 1 ? A1 : A0; bf16x8* const An = N & 1 ? A0 : A1;
    bf16x8* const Bc = N & 1 ? By : Bx; bf16x8* const Bn = N & 1 ? Bx : By;
    const f32x4 zero = {0, 0, 0, 0};
    if (N == 34) { xa = ldx2(5 + w, 0); xb = ldx2(5 + w, 1); }
    if (NN > 0) {
      if (N == 32) WAIT_V(0);
      if (N == 0) WAIT_L0(); else WAIT_L3();
      BAR();
      if (N == 32 && tid == 0) __hip_atomic_store(flags + (FL_OUT + 2 * tt) * 32 + fs, 1u, __ATOMIC_RELAXED, __HIP_MEMORY_SCOPE_AGENT);
      SCHED();
      if (N == 0) { rdB(Bc, 0); SCHED(); }
      Bn[0] = lds_frag(lds + vb + kn * 4096);
      rdA(An, NN, (N + 1) & 3);
#pragma unroll
      for (int m = 1; m < 4; m++) Bn[m] = lds_frag(lds + vb + kn * 4096 + m * 1024);
    }
    if (PUT >= 0) put(PUT >> 1, PUT & 1);
#pragma unroll
    for (int m = 0; m < 4; m++)
#pragma unroll
      for (int j = 0; j < NC; j++) acc[m][J0 + j] = mfma(Ac[j], Bc[m], FIRST ? zero : acc[m][J0 + j]);
    if (NN > 0) {
      constexpr int NG = 2 * NN + 4;
#pragma unroll
      for (int i = 0; i < NG; i++) {
        SGB(0x008, 1); SGB(0x100, 1);
        if (PUT >= 0 && i >= NG - 4) SGB(0x002, 1);
        if (PUT >= 0 && i == NG - 1) SGB(0x040, 1);
      }
      SGB(0x008, 4 * NC - NG);
    }
    SCHED();
  };
  step2(IC<0>{}, 1);
#pragma nounroll
  for (int n = 1; n < 17; n += 4) static_for<4>([&](auto I) { step2(IC<1 + I>{}, n + 1 + I); });
  step2(IC<17>{}, 0);
  static_for<18>([&](auto I) { step2(IC<18 + I>{}, 1 + I); });
#pragma unroll
  for (int j = 5; j < 9; j++) { put(j, 0); put(j, 1); }
  WAIT_V(0);
  handoff(sw + 10, lane, flags + (FL_OUT + 2 * tt + 1) * 32 + fs, flags + (FL_OUT + 2 * tt + 1) * 32 + (lane & 7), [](bool) {});
  u32x4 p[8];
  gather(p, 5 + w);
  finish(p, xa, xb, 5 + w);
}

extern "C" void kernel_launch(void* const* d_in, const int* in_sizes, int n_in, void* d_out, int out_size, void* d_ws,
                              size_t ws_size, hipStream_t stream) {
  const u16* x = (const u16*)d_in[0];
  char* ws = (char*)d_ws;
  k0_ln_mod<<<472, 320, 0, stream>>>(x, (const u16*)d_in[8], (const u16*)d_in[9], (const u16*)d_in[1], (const u16*)d_in[2],
                                     (const u16*)d_in[3], (const u16*)d_in[4], ws);
  kf_main<<<256, NT, 0, stream>>>((const u16*)d_in[1], (const u16*)d_in[2], x, (const u16*)d_in[3], (const u16*)d_in[4],
                                  (const u16*)d_in[6], (const u16*)d_in[7], ws, (u16*)d_out);
}
```
